```python
import torch

def _softmax(x, axis=-1):
    return torch.softmax(x, dim=axis)
def _gelu(x):
    return torch.nn.functional.gelu(x, approximate="tanh")
def _silu(x):
    return torch.nn.functional.silu(x)
def _sigmoid(x):
    return torch.sigmoid(x)
def _layernorm(x, axis=-1, eps=1e-5):
    m = x.mean(dim=axis, keepdim=True)
    v = x.var(dim=axis, keepdim=True, unbiased=False)
    return (x - m) / torch.sqrt(v + eps)
def _scatter_add(tgt, idx, src, axis=0):
    return tgt.clone().index_add_(axis, idx, src)
def _segment_sum(src, seg, n):
    out = torch.zeros((n,) + src.shape[1:], dtype=src.dtype)
    return out.index_add_(0, seg, src)
def _pad(x, pw):
    flat = tuple(v for pair in reversed(pw) for v in pair)
    return torch.nn.functional.pad(x, flat)

kernel_name = 'gated_mix__softmax_full_mha__mixer_s_mlp__prompt'

def setup_inputs():

    _w = (lambda a: (lambda t: t.to(torch.bfloat16) if t.is_floating_point() else t)(torch.as_tensor(a)))
    def _A_setup():
        B, S, D = 1, 2048, 512
        return {
            "x": _w((lambda *s: torch.randn(*s, dtype=torch.bfloat16) * 0.05)(B, S, D)),
            "wqkv": _w((lambda *s: torch.randn(*s, dtype=torch.bfloat16) * 1.6)(D, 3 * D)),
            "wo": _w((lambda *s: torch.randn(*s, dtype=torch.bfloat16) * 0.1)(D, D)),
        }
    _A_in = _A_setup()

    def _B_setup():
        B, S, D, F = 1, 2048, 512, 1792
        return {
            "x":  _w((lambda *s: torch.randn(*s, dtype=torch.bfloat16) * 0.05)(B, S, D)),
            "w1": _w((lambda *s: torch.randn(*s, dtype=torch.bfloat16) * 3.2)(D, D) * (D ** -0.5)),
            "w2": _w((lambda *s: torch.randn(*s, dtype=torch.bfloat16) * 3.2)(D, D) * (D ** -0.5)),
            "w3": _w((lambda *s: torch.randn(*s, dtype=torch.bfloat16) * 3.2)(D, F) * (D ** -0.5)),
            "w4": _w((lambda *s: torch.randn(*s, dtype=torch.bfloat16) * 1.6)(F, D) * (F ** -0.5)),
        }
    _B_in = _B_setup()

    return {
        "A_x": _A_in["x"],
        "A_wqkv": _A_in["wqkv"],
        "A_wo": _A_in["wo"],
        "B_x": _B_in["x"],
        "B_w1": _B_in["w1"],
        "B_w2": _B_in["w2"],
        "B_w3": _B_in["w3"],
        "B_w4": _B_in["w4"],
        "gate_w": _w((lambda *s: torch.randn(*s, dtype=torch.bfloat16) * 0.05)(512) * 16.0),
    }

def reference(A_x, A_wqkv, A_wo, B_x, B_w1, B_w2, B_w3, B_w4, gate_w):
    def _A_ref(x, wqkv, wo):
        B, S, D = x.shape
        H = 32; Dh = D // H
        qkv = (lambda x, *d: x.permute(*d))((x @ wqkv).reshape(B, S, 3, H, Dh), 2, 0, 3, 1, 4)
        q, k, v = qkv[0], qkv[1], qkv[2]
        scale = Dh ** -0.5
        scores = torch.einsum("bhqd,bhkd->bhqk", q, k) * scale
        p = _softmax(scores, axis=-1)
        o = torch.einsum("bhqk,bhkd->bhqd", p, v)
        return (lambda x, *d: x.permute(*d))(o, 0, 2, 1, 3).reshape(B, S, D) @ wo

    def _B_ref(x, w1, w2, w3, w4):
        B, S, D = x.shape
        h = _gelu(_layernorm(x, axis=-1) @ w1)
        c = D // 4
        g0 = _pad(h[:, :, 0*c:1*c], ((0, 0), (1, 0), (0, 0)))[:, :S, :]
        g1 = _pad(h[:, :, 1*c:2*c], ((0, 0), (0, 1), (0, 0)))[:, 1:, :]
        g2 = _pad(h[:, :, 2*c:3*c], ((0, 0), (2, 0), (0, 0)))[:, :S, :]
        g3 = _pad(h[:, :, 3*c:   ], ((0, 0), (0, 2), (0, 0)))[:, 2:, :]
        hs = torch.concatenate([g0, g1, g2, g3], axis=-1) @ w2
        x1 = x + hs
        h2 = _gelu(_layernorm(x1, axis=-1) @ w3) @ w4
        return x1 + h2

    x = A_x
    g = _sigmoid((x * gate_w).sum(-1, keepdims=True))
    a = _A_ref(x, A_wqkv, A_wo)
    b = _B_ref(x, B_w1, B_w2, B_w3, B_w4)
    return g * a + (1 - g) * b
```

```cpp
#include <hip/hip_runtime.h>
#include <hip/hip_bf16.h>

typedef __attribute__((ext_vector_type(8))) __bf16 bf16x8;
typedef __attribute__((ext_vector_type(16))) __bf16 bf16x16;
typedef __attribute__((ext_vector_type(16))) float f32x16;
typedef __attribute__((ext_vector_type(8))) float f32x8;
typedef __attribute__((ext_vector_type(4))) float f32x4;
typedef __attribute__((ext_vector_type(2))) float f32x2;
typedef __attribute__((ext_vector_type(4))) unsigned u32x4;
typedef __attribute__((ext_vector_type(2))) unsigned u32x2;
typedef __attribute__((ext_vector_type(4))) short s16x4;
typedef __attribute__((address_space(3))) char* lds_p;
typedef __amdgpu_buffer_rsrc_t rsrc_t;
#define DI __device__ __forceinline__

constexpr int S = 2048, D = 512, F = 1792, NH = 32;
constexpr size_t WS_Q = 0, WS_K = WS_Q + (2u << 20), WS_V = WS_K + (2u << 20), WS_H = WS_V + (2u << 20),
                 WS_X1 = WS_H + (2u << 20), WS_O = WS_X1 + (2u << 20), WS_U = WS_O + (2u << 20),
                 WS_ST = WS_U + (size_t)S * F * 2,
                 WS_G = WS_ST + (size_t)S * 8 * 8,
                 WS_CS3 = WS_G + S * 4,
                 WS_END = WS_CS3 + 8 * F * 4;
constexpr float QSCALE = 0.25f * 1.4426950408889634f;
constexpr float LN_EPS = 1e-5f;

DI rsrc_t mk_rsrc(const void* p, unsigned bytes) { return __builtin_amdgcn_make_buffer_rsrc((void*)p, (short)0, (int)bytes, 0x00020000); }
DI unsigned rfl(unsigned x) { return __builtin_amdgcn_readfirstlane(x); }
DI void dma16(rsrc_t r, unsigned voff, unsigned soff, unsigned ldst) {
  asm volatile("s_mov_b32 m0, %0\n\ts_nop 0\n\tbuffer_load_dwordx4 %1, %2, %3 offen lds" ::"s"(ldst), "v"(voff), "s"(r), "s"(soff) : "memory");
}
#define WAITV(n) asm volatile("s_waitcnt vmcnt(" #n ")" ::: "memory")
#define WAITV_BAR(n) asm volatile("s_waitcnt vmcnt(" #n ") lgkmcnt(0)\n\ts_barrier" ::: "memory")
#define LBAR() asm volatile("s_waitcnt lgkmcnt(0)\n\ts_barrier" ::: "memory")
#define SBAR() __builtin_amdgcn_sched_barrier(0)
template <int N> DI void waitv() { asm volatile("s_waitcnt vmcnt(%0)" ::"i"(N) : "memory"); }
template <int N> DI void waitv_bar() { asm volatile("s_waitcnt vmcnt(%0) lgkmcnt(0)\n\ts_barrier" ::"i"(N) : "memory"); }

DI u32x4 lds128(lds_p p) { return *(__attribute__((address_space(3))) u32x4*)p; }
DI u32x2 ldstr(lds_p p) { return __builtin_bit_cast(u32x2, __builtin_amdgcn_ds_read_tr16_b64_v4i16((__attribute__((address_space(3))) s16x4*)p)); }
DI u32x4 wfrag(lds_p p, int rowpitch4) { u32x2 a = ldstr(p), b = ldstr(p + rowpitch4); return (u32x4){a[0], a[1], b[0], b[1]}; }
DI f32x16 mfma(u32x4 a, u32x4 b, f32x16 c) { return __builtin_amdgcn_mfma_f32_32x32x16_bf16(__builtin_bit_cast(bf16x8, a), __builtin_bit_cast(bf16x8, b), c, 0, 0, 0); }
DI f32x16 smf(u32x4 a, f32x8 b, f32x16 c, int idx) { return __builtin_amdgcn_smfmac_f32_32x32x32_bf16(__builtin_bit_cast(bf16x8, a), __builtin_bit_cast(bf16x16, b), c, idx, 0, 0); }
DI f32x8 lo8(f32x16 v) { return __builtin_shufflevector(v, v, 0, 1, 2, 3, 4, 5, 6, 7); }
DI f32x8 hi8(f32x16 v) { return __builtin_shufflevector(v, v, 8, 9, 10, 11, 12, 13, 14, 15); }
DI unsigned pk2(float a, float b) { typedef __attribute__((ext_vector_type(2))) __bf16 b2; b2 r = {(__bf16)a, (__bf16)b}; return __builtin_bit_cast(unsigned, r); }
DI u32x4 pk8(const float* v) { return (u32x4){pk2(v[0], v[1]), pk2(v[2], v[3]), pk2(v[4], v[5]), pk2(v[6], v[7])}; }
DI float bflo(unsigned u) { return __uint_as_float(u << 16); }
DI float bfhi(unsigned u) { return __uint_as_float(u & 0xffff0000u); }
DI float xhalf_sum(float v) { auto r = __builtin_amdgcn_permlane32_swap(__float_as_uint(v), __float_as_uint(v), false, false); return __uint_as_float(r[0]) + __uint_as_float(r[1]); }
DI float xhalf_max(float v) { auto r = __builtin_amdgcn_permlane32_swap(__float_as_uint(v), __float_as_uint(v), false, false); return fmaxf(__uint_as_float(r[0]), __uint_as_float(r[1])); }
DI int swap23(int x) { return (x & ~12) | ((x & 4) << 1) | ((x & 8) >> 1); }
DI float gelu(float x) {
  constexpr float K1 = -2.0f * 0.7978845608028654f * 1.4426950408889634f, K3 = K1 * 0.044715f;
  float e = __builtin_amdgcn_exp2f(x * __builtin_fmaf(x * x, K3, K1));
  return x * __builtin_amdgcn_rcpf(1.0f + e);
}
DI void st128(rsrc_t r, unsigned off, u32x4 v) { __builtin_amdgcn_raw_buffer_store_b128(v, r, off, 0, 0); }
DI u32x4 ld128(rsrc_t r, unsigned off) { return __builtin_amdgcn_raw_buffer_load_b128(r, off, 0, 0); }

#define ONES4 ((u32x4){0x3f803f80u, 0x3f803f80u, 0x3f803f80u, 0x3f803f80u})

constexpr int K1_SLOT = 32768, K1_X = 0, K1_W = 16384, K1_SIDE = 4 * K1_SLOT, K1_GW = K1_SIDE + 8192, K1_LDS = K1_GW + 1024;
enum { T_Q = 0, T_QG = 1, T_K = 2, T_V = 3, T_H = 4 };

template <int TYPE>
DI void k1_body(lds_p sm, int tm, int strip, int wm, int sidx, int lane, rsrc_t rx, rsrc_t rw, unsigned xv0, unsigned xv1, unsigned wv0, unsigned wv1,
                unsigned wstep, unsigned ldsx, unsigned ldsw, char* ws) {
  const int rho = lane & 31, hh = lane >> 5, tok = swap23(rho);
  const int row = wm * 32 + tok, fx = (row >> 1) & 7;
  lds_p xa[4];
#pragma unroll
  for (int ks = 0; ks < 4; ++ks) xa[ks] = sm + K1_X + row * 128 + (((2 * ks + hh) ^ fx) << 4);
  const int q4 = (lane & 15) >> 2, p4 = lane & 3, cb = (lane >> 4) & 1;
  const int n0 = (TYPE == T_V) ? (16 * cb + 8 * (p4 & 1) + 4 * (p4 >> 1)) : (16 * (p4 & 1) + 8 * cb + 4 * (p4 >> 1));
  const int wo_ = (8 * hh + q4) * 128 + 2 * n0, sw = ((q4 >> 1) & 1) << 6;
  lds_p wa0 = sm + K1_W + sidx * 8192 + wo_ + sw, wa1 = sm + K1_W + sidx * 8192 + wo_ + (sw ^ 64);
  lds_p ga = sm + K1_GW + hh * 16;
  f32x16 acc0 = {}, acc1 = {}, ex0 = {}, ex1 = {};
  float sx = 0.f, sxx = 0.f;
  const u32x4 ones = ONES4;
#pragma unroll
  for (int s = 0; s < 8; ++s) {
    if (s == 0) WAITV_BAR(12); else if (s < 6) WAITV_BAR(8); else if (s == 6) WAITV_BAR(4); else WAITV_BAR(0);
    if (s >= 1 && s + 3 <= 7) {
      const int t = s + 3, sl = (t & 3) * K1_SLOT;
      dma16(rx, xv0, t * 128, ldsx + sl); dma16(rx, xv1, t * 128, ldsx + sl + 1024);
      dma16(rw, wv0, t * wstep, ldsw + sl); dma16(rw, wv1, t * wstep, ldsw + sl + 1024);
    }
    const int sl = (s & 3) * K1_SLOT;
#pragma unroll
    for (int ks = 0; ks < 4; ++ks) {
      u32x4 xf = lds128(xa[ks] + sl);
      u32x4 w0 = wfrag(wa0 + sl + ks * 2048, 512), w1 = wfrag(wa1 + sl + ks * 2048, 512);
      if (TYPE == T_V) { acc0 = mfma(xf, w0, acc0); acc1 = mfma(xf, w1, acc1); }
      else { acc0 = mfma(w0, xf, acc0); acc1 = mfma(w1, xf, acc1); }
      if (TYPE == T_QG) { u32x4 gf = lds128(ga + (s * 4 + ks) * 32); ex0 = mfma(gf, xf, ex0); }
      if (TYPE == T_H) {
        ex0 = mfma(w0, ones, ex0); ex1 = mfma(w1, ones, ex1);
#pragma unroll
        for (int i = 0; i < 4; ++i) { float a = bflo(xf[i]), b = bfhi(xf[i]); sx += a; sxx = __builtin_fmaf(a, a, sxx); sx += b; sxx = __builtin_fmaf(b, b, sxx); }
        asm volatile("" : "+v"(sx), "+v"(sxx));
      }
    }
  }
  const int tile = tm * 4 + wm;
  if (TYPE == T_Q || TYPE == T_QG || TYPE == T_K) {
    rsrc_t ro = mk_rsrc(ws + (TYPE == T_K ? WS_K : WS_Q), 2u << 20);
    const float sc = (TYPE == T_K) ? 1.0f : QSCALE;
    const unsigned o = (strip * 4 + hh) * 65536 + tile * 1024 + tok * 16;
    float v[16];
#pragma unroll
    for (int r = 0; r < 16; ++r) v[r] = acc0[r] * sc;
    st128(ro, o, pk8(v)); st128(ro, o + 512, pk8(v + 8));
#pragma unroll
    for (int r = 0; r < 16; ++r) v[r] = acc1[r] * sc;
    st128(ro, o + 131072, pk8(v)); st128(ro, o + 131072 + 512, pk8(v + 8));
    if (TYPE == T_QG && hh == 0) ((float*)(ws + WS_G))[tile * 32 + tok] = __builtin_amdgcn_rcpf(1.0f + __builtin_amdgcn_exp2f(-1.4426950408889634f * ex0[0]));
  } else if (TYPE == T_V) {
    rsrc_t ro = mk_rsrc(ws + WS_V, 2u << 20);
    const unsigned o = (strip * 4 + (rho >> 4)) * 65536 + tile * 1024 + hh * 256 + (rho & 15) * 16;
    float v[16];
#pragma unroll
    for (int r = 0; r < 16; ++r) v[r] = acc0[r];
    st128(ro, o, pk8(v)); st128(ro, o + 512, pk8(v + 8));
#pragma unroll
    for (int r = 0; r < 16; ++r) v[r] = acc1[r];
    st128(ro, o + 131072, pk8(v)); st128(ro, o + 131072 + 512, pk8(v + 8));
  } else {
    rsrc_t ro = mk_rsrc(ws + WS_H, 2u << 20);
    sx = xhalf_sum(sx); sxx = xhalf_sum(sxx);
    const float mean = sx * (1.0f / D), var = fmaxf(sxx * (1.0f / D) - mean * mean, 0.f), rstd = __builtin_amdgcn_rsqf(var + LN_EPS);
    const int grp = strip >> 1, sh = (grp == 0) ? 1 : (grp == 1) ? -1 : (grp == 2) ? 2 : -2;
    const int dst = tile * 32 + tok + sh, dw = dst & (S - 1);
    const bool ok = (dst == dw);
    const unsigned o = (dw >> 5) * 32768 + (strip * 4 + hh) * 1024 + (dw & 31) * 16;
    float v[16];
#pragma unroll
    for (int r = 0; r < 16; ++r) { float y = gelu(rstd * __builtin_fmaf(-mean, ex0[r], acc0[r])); v[r] = ok ? y : 0.f; }
    st128(ro, o, pk8(v)); st128(ro, o + 512, pk8(v + 8));
#pragma unroll
    for (int r = 0; r < 16; ++r) { float y = gelu(rstd * __builtin_fmaf(-mean, ex1[r], acc1[r])); v[r] = ok ? y : 0.f; }
    st128(ro, o + 2048, pk8(v)); st128(ro, o + 2048 + 512, pk8(v + 8));
  }
}

__global__ void __launch_bounds__(512, 2) k1_kernel(const char* x, const char* wqkv, const char* w1, const char* w3, const char* gw, char* ws) {
  __shared__ __attribute__((aligned(16))) char smem[K1_LDS];
  lds_p sm = (lds_p)smem;
  const int b = blockIdx.x, xc = b & 7, i = b >> 3;
  const int tm = (xc >> 2) * 8 + (i >> 2), j = (xc & 3) * 4 + (i & 3);
  const int lane = threadIdx.x & 63, w = rfl(threadIdx.x >> 6), wm = w & 3, sidx = w >> 2;
  const unsigned lds0 = (unsigned)(size_t)sm;
  rsrc_t rx = mk_rsrc(x, S * D * 2);
  rsrc_t r3 = mk_rsrc(w3, D * F * 2);
  const int cstrip = b % 28, crow = b / 28;
  if (w == 0 && b < 224) {
    const unsigned v = ((lane >> 3) * F + cstrip * 64) * 2 + (((lane & 7) ^ (((lane >> 4) & 1) << 2)) << 4);
#pragma unroll
    for (int t = 0; t < 8; ++t) dma16(r3, v, (crow * 64 + t * 8) * F * 2, lds0 + K1_SIDE + t * 1024);
  }
  if (w == 1 && j == 0) { rsrc_t rg = mk_rsrc(gw, D * 2); dma16(rg, lane * 16, 0, lds0 + K1_GW); }
  const int xr = 16 * w + (lane >> 3), pc = lane & 7;
  const unsigned xv0 = (tm * 128 + xr) * 1024 + ((pc ^ ((xr >> 1) & 7)) << 4);
  const unsigned xv1 = (tm * 128 + xr + 8) * 1024 + ((pc ^ (((xr + 8) >> 1) & 7)) << 4);
  const bool isH = (j >= 8 && sidx == 1);
  const int strip = j & 7;
  const int ld = isH ? D : 3 * D, col0 = isH ? strip * 64 : (j < 8 ? sidx * 512 : 1024) + strip * 64;
  rsrc_t rw = mk_rsrc(isH ? w1 : wqkv, D * ld * 2);
  const int kr = 16 * wm + (lane >> 3);
  const unsigned wv0 = (kr * ld + col0) * 2 + ((pc ^ (((kr >> 1) & 1) << 2)) << 4), wv1 = wv0 + 8 * ld * 2;
  const unsigned wstep = 64 * ld * 2;
  const unsigned ldsx = lds0 + K1_X + w * 2048, ldsw = lds0 + K1_W + w * 2048;
#pragma unroll
  for (int t = 0; t < 4; ++t) {
    dma16(rx, xv0, t * 128, ldsx + t * K1_SLOT); dma16(rx, xv1, t * 128, ldsx + t * K1_SLOT + 1024);
    dma16(rw, wv0, t * wstep, ldsw + t * K1_SLOT); dma16(rw, wv1, t * wstep, ldsw + t * K1_SLOT + 1024);
  }
  if (j < 8) {
    if (sidx == 1) k1_body<T_K>(sm, tm, strip, wm, sidx, lane, rx, rw, xv0, xv1, wv0, wv1, wstep, ldsx, ldsw, ws);
    else if (j == 0) k1_body<T_QG>(sm, tm, strip, wm, sidx, lane, rx, rw, xv0, xv1, wv0, wv1, wstep, ldsx, ldsw, ws);
    else k1_body<T_Q>(sm, tm, strip, wm, sidx, lane, rx, rw, xv0, xv1, wv0, wv1, wstep, ldsx, ldsw, ws);
  } else {
    if (sidx == 1) k1_body<T_H>(sm, tm, strip, wm, sidx, lane, rx, rw, xv0, xv1, wv0, wv1, wstep, ldsx, ldsw, ws);
    else k1_body<T_V>(sm, tm, strip, wm, sidx, lane, rx, rw, xv0, xv1, wv0, wv1, wstep, ldsx, ldsw, ws);
  }
  if (w == 0 && b < 224) {
    const int hh = lane >> 5, q4 = (lane & 15) >> 2, p4 = lane & 3, cb = (lane >> 4) & 1;
    const int n0 = 16 * (p4 & 1) + 8 * cb + 4 * (p4 >> 1), wo_ = (8 * hh + q4) * 128 + 2 * n0, sw = ((q4 >> 1) & 1) << 6;
    f32x16 c0 = {}, c1 = {};
#pragma unroll
    for (int ks = 0; ks < 4; ++ks) {
      c0 = mfma(wfrag(sm + K1_SIDE + wo_ + sw + ks * 2048, 512), ONES4, c0);
      c1 = mfma(wfrag(sm + K1_SIDE + wo_ + (sw ^ 64) + ks * 2048, 512), ONES4, c1);
    }
    if ((lane & 31) == 0) {
      f32x4* o = (f32x4*)(ws + WS_CS3 + (crow * F + cstrip * 64 + hh * 16) * 4);
#pragma unroll
      for (int g = 0; g < 4; ++g) { o[g] = (f32x4){c0[4 * g], c0[4 * g + 1], c0[4 * g + 2], c0[4 * g + 3]}; o[8 + g] = (f32x4){c1[4 * g], c1[4 * g + 1], c1[4 * g + 2], c1[4 * g + 3]}; }
    }
  }
}

constexpr int AT_SLOT = 16384, AT_ONES = 4 * AT_SLOT, AT_LDS = AT_ONES + 8192;

DI void exp4(f32x16& s, int i) {
#pragma unroll
  for (int k = 0; k < 4; ++k) s[i + k] = __builtin_amdgcn_exp2f(s[i + k]);
}
DI float max16(const f32x16& s) {
  float a = fmaxf(fmaxf(s[0], s[1]), s[2]), b = fmaxf(fmaxf(s[3], s[4]), s[5]);
  a = fmaxf(fmaxf(a, s[6]), s[7]); b = fmaxf(fmaxf(b, s[8]), s[9]); a = fmaxf(fmaxf(a, s[10]), s[11]); b = fmaxf(fmaxf(b, s[12]), s[13]);
  return fmaxf(fmaxf(a, s[14]), fmaxf(b, s[15]));
}
DI f32x16 attn_slow(const char* kg, const char* vg, u32x4 qf, int lane) {
  const int rho = lane & 31, hh = lane >> 5;
  rsrc_t rk = mk_rsrc(kg, 65536), rv = mk_rsrc(vg, 65536);
  f32x16 o = {}; float m = -INFINITY;
  for (int t = 0; t < 64; ++t) {
    u32x4 kf = ld128(rk, t * 1024 + lane * 16);
    u32x4 v0 = ld128(rv, t * 1024 + hh * 256 + (rho & 15) * 16), v1 = ld128(rv, t * 1024 + 512 + hh * 256 + (rho & 15) * 16);
    if (rho >= 16) { v0 = ONES4; v1 = ONES4; }
    f32x16 z = {}; f32x16 s = mfma(kf, qf, z);
    const float mn = fmaxf(m, xhalf_max(max16(s))), al = __builtin_amdgcn_exp2f(m - mn);
    m = mn;
#pragma unroll
    for (int r = 0; r < 16; ++r) { o[r] *= al; s[r] = __builtin_amdgcn_exp2f(s[r] - mn); }
    o = smf(v0, lo8(s), o, 0xDDDD); o = smf(v1, hi8(s), o, 0xDDDD);
  }
  return o;
}
DI bool attn_ok(const f32x16& o) {
  float t = 0.f;
#pragma unroll
  for (int r = 0; r < 8; ++r) t = __builtin_fmaf(o[r], 0.f, t);
  return (o[8] >= 0x1p-40f) && (o[8] <= 0x1p100f) && (t == 0.f);
}
DI void attn_store(const f32x16& o, rsrc_t ro, unsigned off) {
  const float rl = 1.0f / o[8];
  float v[8];
#pragma unroll
  for (int r = 0; r < 8; ++r) v[r] = o[r] * rl;
  st128(ro, off, pk8(v));
}

DI void attn_body(lds_p sm, int head, int qb, char* ws) {
  const int lane = threadIdx.x & 63, w = rfl(threadIdx.x >> 6) & 3, rho = lane & 31, hh = lane >> 5;
  const unsigned lds0 = (unsigned)(size_t)sm;
  rsrc_t rk = mk_rsrc(ws + WS_K + head * 65536, 65536), rv = mk_rsrc(ws + WS_V + head * 65536, 65536), rq = mk_rsrc(ws + WS_Q + head * 65536, 65536);
  const unsigned lv = lane * 16 + w * 2048, ld = lds0 + w * 2048;
#define AT_STAGE(c) do { const unsigned sl_ = ld + ((c) & 3) * AT_SLOT; dma16(rk, lv, (c) * 8192, sl_); dma16(rk, lv, (c) * 8192 + 1024, sl_ + 1024); \
    dma16(rv, lv, (c) * 8192, sl_ + 8192); dma16(rv, lv, (c) * 8192 + 1024, sl_ + 8192 + 1024); } while (0)
  const int qt = qb * 8 + w * 2;
  u32x4 qA = ld128(rq, qt * 1024 + lane * 16), qB = ld128(rq, qt * 1024 + 1024 + lane * 16);
  asm volatile("" : "+v"(qA), "+v"(qB));
  AT_STAGE(0); AT_STAGE(1); AT_STAGE(2); AT_STAGE(3);
  {
    __attribute__((address_space(3))) u32x4* p = (__attribute__((address_space(3))) u32x4*)(sm + AT_ONES) + (threadIdx.x & 255);
    p[0] = ONES4; p[256] = ONES4;
  }
  WAITV_BAR(12);
  lds_p ka = sm + lane * 16;
  lds_p va = (rho < 16) ? sm + 8192 + hh * 256 + rho * 16 : sm + AT_ONES;
  const int vstep = (rho < 16) ? AT_SLOT : 0;
  const int idx = 0xDDDD;
  u32x4 kf = lds128(ka);
  f32x16 negm, sA, sB, oA = {}, oB = {};
  { f32x16 z = {}; sA = mfma(kf, qA, z); sB = mfma(kf, qB, z);
    const float m = xhalf_max(fmaxf(max16(sA), max16(sB)));
#pragma unroll
    for (int r = 0; r < 16; ++r) negm[r] = -m; }
  asm volatile("" : "+v"(negm));
  sA = mfma(kf, qA, negm);
  u32x4 v0 = lds128(va), v1 = lds128(va + 512);
  for (int c = 0; c < 8; ++c) {
    const int nsl = ((c + 1) & 3) * AT_SLOT;
    lds_p kn = sm + lane * 16 + nsl;
    lds_p vn = va + (((c + 1) & 3) - (c & 3)) * vstep;
#pragma unroll
    for (int t = 0; t < 8; ++t) {
      exp4(sA, 0); SBAR();
      sB = mfma(kf, qB, negm); SBAR();
      if (t == 7) {
        if (c < 5) WAITV_BAR(4); else if (c < 7) WAITV_BAR(0);
        if (c >= 1 && c + 3 < 8) AT_STAGE(c + 3);
      }
      if (t < 7) kf = lds128(ka + (t + 1) * 1024); else if (c < 7) kf = lds128(kn);
      SBAR();
      exp4(sA, 4); SBAR();
      oA = smf(v0, lo8(sA), oA, idx); SBAR();
      exp4(sA, 8); exp4(sA, 12); SBAR();
      oA = smf(v1, hi8(sA), oA, idx); SBAR();
      exp4(sB, 0); SBAR();
      sA = mfma(kf, qA, negm); SBAR();
      exp4(sB, 4); SBAR();
      oB = smf(v0, lo8(sB), oB, idx); SBAR();
      if (t < 7) v0 = lds128(va + (t + 1) * 1024); else if (c < 7) v0 = lds128(vn);
      SBAR();
      exp4(sB, 8); exp4(sB, 12); SBAR();
      oB = smf(v1, hi8(sB), oB, idx); SBAR();
      if (t < 7) v1 = lds128(va + (t + 1) * 1024 + 512); else if (c < 7) v1 = lds128(vn + 512);
      SBAR();
    }
    ka = kn; va = vn;
  }
#undef AT_STAGE
  if (__builtin_expect(!__all(attn_ok(oA) && attn_ok(oB)), 0)) {
#pragma unroll 1
    for (int i = 0; i < 2; ++i) {
      f32x16 o = attn_slow(ws + WS_K + head * 65536, ws + WS_V + head * 65536, i ? qB : qA, lane);
      if (i) oB = o; else oA = o;
    }
  }
  rsrc_t ro = mk_rsrc(ws + WS_O, 2u << 20);
  attn_store(oA, ro, qt * 32768 + head * 1024 + lane * 16);
  attn_store(oB, ro, qt * 32768 + 32768 + head * 1024 + lane * 16);
}

__global__ void __launch_bounds__(256, 2) attn_kernel(char* ws) {
  __shared__ __attribute__((aligned(16))) char smem[AT_LDS];
  const int b = blockIdx.x, xc = b & 7, i = b >> 3;
  attn_body((lds_p)smem, xc * 4 + (i >> 3), i & 7, ws);
}

constexpr int SK_DEPTH = 4, SK_WAVE = SK_DEPTH * 4096, SK_LDS = 4 * SK_WAVE;

struct SkAcc { f32x16 a[2][2]; };
DI void sk_wave(SkAcc& acc, lds_p ring, const char* xp, unsigned xbytes, unsigned xoff, unsigned xmt, const char* wp, unsigned wbytes, unsigned woff, unsigned wld, int nsteps, int lane) {
  rsrc_t rx = mk_rsrc(xp, xbytes), rw = mk_rsrc(wp, wbytes);
  const unsigned l0 = rfl((unsigned)(size_t)ring);
  const unsigned xv = lane * 16;
  const int kr = lane >> 3;
  const unsigned wv = kr * wld + (((lane & 7) ^ (((kr >> 1) & 1) << 2)) << 4);
  const int hh = lane >> 5, q4 = (lane & 15) >> 2, p4 = lane & 3, cb = (lane >> 4) & 1;
  const int wo_ = 2048 + (8 * hh + q4) * 128 + 2 * (16 * (p4 & 1) + 8 * cb + 4 * (p4 >> 1)), sw = ((q4 >> 1) & 1) << 6;
  lds_p xa = ring + lane * 16, wa0 = ring + wo_ + sw, wa1 = ring + wo_ + (sw ^ 64);
#define SK_STAGE(k, sl) do { const unsigned xo_ = xoff + (k) * 1024, wo2_ = woff + (k) * 16 * wld, d_ = l0 + (sl) * 4096; \
    dma16(rx, xv, xo_, d_); dma16(rx, xv, xo_ + xmt, d_ + 1024); dma16(rw, wv, wo2_, d_ + 2048); dma16(rw, wv, wo2_ + 8 * wld, d_ + 3072); } while (0)
  SK_STAGE(0, 0); SK_STAGE(1, 1); SK_STAGE(2, 2); SK_STAGE(3, 3);
  WAITV(12);
  u32x4 x0 = lds128(xa), x1 = lds128(xa + 1024), w0 = wfrag(wa0, 512), w1 = wfrag(wa1, 512);
  for (int k0 = 0; k0 < nsteps; k0 += 4) {
    const bool more = k0 + 4 < nsteps;
    rx = mk_rsrc(xp, more ? xbytes : 0u); rw = mk_rsrc(wp, more ? wbytes : 0u);
#pragma unroll
    for (int u = 0; u < 4; ++u) {
      const int k = k0 + u;
      {
        u32x4 y0, y1, z0, z1;
        asm volatile("s_waitcnt vmcnt(8) lgkmcnt(0)" : "+v"(x0), "+v"(x1), "+v"(w0), "+v"(w1)::"memory");
        SK_STAGE(k + 4, u);
        const int sn = ((u + 1) & 3) * 4096;
        y0 = lds128(xa + sn); y1 = lds128(xa + sn + 1024); z0 = wfrag(wa0 + sn, 512); z1 = wfrag(wa1 + sn, 512);
        SBAR();
        acc.a[0][0] = mfma(w0, x0, acc.a[0][0]); acc.a[0][1] = mfma(w0, x1, acc.a[0][1]);
        acc.a[1][0] = mfma(w1, x0, acc.a[1][0]); acc.a[1][1] = mfma(w1, x1, acc.a[1][1]);
        SBAR();
        x0 = y0; x1 = y1; w0 = z0; w1 = z1;
      }
    }
  }
  asm volatile("s_waitcnt vmcnt(0) lgkmcnt(0)" ::: "memory");
#undef SK_STAGE
}
DI f32x16 sk_reduce(const SkAcc& acc, lds_p sm, int w, int lane) {
  typedef __attribute__((address_space(3))) f32x4* lp4;
  LBAR();
#pragma unroll
  for (int t = 0; t < 4; ++t) {
    if (t != w) {
      const f32x16& a = acc.a[t >> 1][t & 1];
      lp4 p = (lp4)(sm + (w * 4 + t) * 4096 + lane * 16);
#pragma unroll
      for (int g = 0; g < 4; ++g) p[g * 64] = (f32x4){a[4 * g], a[4 * g + 1], a[4 * g + 2], a[4 * g + 3]};
    }
  }
  LBAR();
  f32x16 r = (w == 0) ? acc.a[0][0] : (w == 1) ? acc.a[0][1] : (w == 2) ? acc.a[1][0] : acc.a[1][1];
#pragma unroll
  for (int o = 1; o < 4; ++o) {
    lp4 p = (lp4)(sm + (((w + o) & 3) * 4 + w) * 4096 + lane * 16);
#pragma unroll
    for (int g = 0; g < 4; ++g) { f32x4 v = p[g * 64]; r[4 * g] += v[0]; r[4 * g + 1] += v[1]; r[4 * g + 2] += v[2]; r[4 * g + 3] += v[3]; }
  }
  return r;
}

DI void w2_body(lds_p sm, int tm, int tn, const char* x, const char* w2, char* ws) {
  const int lane = threadIdx.x & 63, w = rfl(threadIdx.x >> 6) & 3, hh = lane >> 5, m = lane & 31;
  const int nb = w >> 1, mb = w & 1, row = tm * 64 + mb * 32 + m, col = tn * 64 + nb * 32 + hh * 16;
  rsrc_t rxin = mk_rsrc(x, S * D * 2);
  u32x4 xa = ld128(rxin, row * 1024 + col * 2), xb = ld128(rxin, row * 1024 + col * 2 + 16);
  asm volatile("" : "+v"(xa), "+v"(xb));
  SkAcc acc = {};
  sk_wave(acc, sm + w * SK_WAVE, ws + WS_H, 2u << 20, tm * 65536 + w * 8192, 32768, w2, D * D * 2, (w * 128 * D + tn * 64) * 2, D * 2, 8, lane);
  f32x16 r = sk_reduce(acc, sm, w, lane);
  float v[16];
#pragma unroll
  for (int i = 0; i < 4; ++i) { v[2 * i] = r[2 * i] + bflo(xa[i]); v[2 * i + 1] = r[2 * i + 1] + bfhi(xa[i]); v[8 + 2 * i] = r[8 + 2 * i] + bflo(xb[i]); v[9 + 2 * i] = r[9 + 2 * i] + bfhi(xb[i]); }
  const u32x4 pa = pk8(v), pb = pk8(v + 8);
  rsrc_t ro = mk_rsrc(ws + WS_X1, 2u << 20);
  const unsigned o = (row >> 5) * 32768 + (col >> 4) * 1024 + m * 16;
  st128(ro, o, pa); st128(ro, o + 512, pb);
  float sx = 0.f, sxx = 0.f;
#pragma unroll
  for (int i = 0; i < 4; ++i) { float a = bflo(pa[i]), b = bfhi(pa[i]), c = bflo(pb[i]), d = bfhi(pb[i]); sx += (a + b) + (c + d);
    sxx = __builtin_fmaf(a, a, sxx); sxx = __builtin_fmaf(b, b, sxx); sxx = __builtin_fmaf(c, c, sxx); sxx = __builtin_fmaf(d, d, sxx); }
  sx = xhalf_sum(sx); sxx = xhalf_sum(sxx);
  __attribute__((address_space(3))) f32x2* sp = (__attribute__((address_space(3))) f32x2*)(sm + 65536 - 4096 + 0);
  if (nb == 1 && hh == 0) sp[mb * 32 + m] = (f32x2){sx, sxx};
  LBAR();
  if (nb == 0 && hh == 0) { f32x2 t = sp[mb * 32 + m]; ((f32x2*)(ws + WS_ST))[row * 8 + tn] = (f32x2){sx + t[0], sxx + t[1]}; }
}
__global__ void __launch_bounds__(256, 2) w2_kernel(const char* x, const char* w2, char* ws) {
  __shared__ __attribute__((aligned(16))) char smem[SK_LDS];
  const int b = blockIdx.x, xc = b & 7, i = b >> 3;
  w2_body((lds_p)smem, xc * 4 + (i >> 3), i & 7, x, w2, ws);
}

DI void fin_body(lds_p sm, int tm, int tn, const char* w4, const char* wo, char* ws, char* out) {
  const int lane = threadIdx.x & 63, w = rfl(threadIdx.x >> 6) & 3, hh = lane >> 5, m = lane & 31;
  const int nb = w >> 1, mb = w & 1, row = tm * 64 + mb * 32 + m, col = tn * 64 + nb * 32 + hh * 16;
  rsrc_t r1 = mk_rsrc(ws + WS_X1, 2u << 20);
  const unsigned o1 = (row >> 5) * 32768 + (col >> 4) * 1024 + m * 16;
  u32x4 xa = ld128(r1, o1), xb = ld128(r1, o1 + 512);
  const float* gp = (const float*)(ws + WS_G);
  float g0 = gp[tm * 64 + m], g1 = gp[tm * 64 + 32 + m];
  asm volatile("" : "+v"(xa), "+v"(xb), "+v"(g0), "+v"(g1));
  SkAcc acc = {};
  if (w < 3) {
    const int k0 = (w == 0) ? 0 : (w == 1) ? 40 : 76, n = (w == 0) ? 40 : 36;
    sk_wave(acc, sm + w * SK_WAVE, ws + WS_U, S * F * 2, tm * 2 * 112 * 1024 + k0 * 1024, 112 * 1024, w4, F * D * 2, (k0 * 16 * D + tn * 64) * 2, D * 2, n, lane);
  } else {
    sk_wave(acc, sm + w * SK_WAVE, ws + WS_O, 2u << 20, tm * 65536, 32768, wo, D * D * 2, tn * 128, D * 2, 32, lane);
  }
  const float f0 = (w < 3) ? 1.0f - g0 : g0, f1 = (w < 3) ? 1.0f - g1 : g1;
#pragma unroll
  for (int r = 0; r < 16; ++r) { acc.a[0][0][r] *= f0; acc.a[1][0][r] *= f0; acc.a[0][1][r] *= f1; acc.a[1][1][r] *= f1; }
  f32x16 r = sk_reduce(acc, sm, w, lane);
  const float gb = 1.0f - (mb ? g1 : g0);
  float v[16];
#pragma unroll
  for (int i = 0; i < 4; ++i) { v[2 * i] = __builtin_fmaf(gb, bflo(xa[i]), r[2 * i]); v[2 * i + 1] = __builtin_fmaf(gb, bfhi(xa[i]), r[2 * i + 1]);
    v[8 + 2 * i] = __builtin_fmaf(gb, bflo(xb[i]), r[8 + 2 * i]); v[9 + 2 * i] = __builtin_fmaf(gb, bfhi(xb[i]), r[9 + 2 * i]); }
  rsrc_t ro = mk_rsrc(out, S * D * 2);
  st128(ro, row * 1024 + col * 2, pk8(v)); st128(ro, row * 1024 + col * 2 + 16, pk8(v + 8));
}
__global__ void __launch_bounds__(256, 2) fin_kernel(const char* w4, const char* wo, char* ws, char* out) {
  __shared__ __attribute__((aligned(16))) char smem[SK_LDS];
  const int b = blockIdx.x, xc = b & 7, i = b >> 3;
  fin_body((lds_p)smem, xc * 4 + (i >> 3), i & 7, w4, wo, ws, out);
}

constexpr int W3_SLOT = 16384, W3_CS = 4 * W3_SLOT, W3_LDS = W3_CS + 512;
DI void w3_body(lds_p sm, int tm, int tn, const char* w3, char* ws) {
  const int tid = threadIdx.x & 255, lane = tid & 63, w = rfl(threadIdx.x >> 6) & 3, hh = lane >> 5, m = lane & 31;
  const unsigned l0 = (unsigned)(size_t)sm;
  const int mt = tm * 4 + w;
  float mean, rstd;
  { const f32x4* sp = (const f32x4*)(ws + WS_ST + (size_t)(mt * 32 + m) * 64 + hh * 32);
    f32x4 a = sp[0], b = sp[1];
    float cs = 0.f;
    if (tid < 128) { const float* cp = (const float*)(ws + WS_CS3) + tn * 128 + tid;
#pragma unroll
      for (int i = 0; i < 8; ++i) cs += cp[i * F]; }
    const float sx = xhalf_sum((a[0] + a[2]) + (b[0] + b[2])), sxx = xhalf_sum((a[1] + a[3]) + (b[1] + b[3]));
    mean = sx * (1.0f / D); rstd = __builtin_amdgcn_rsqf(fmaxf(sxx * (1.0f / D) - mean * mean, 0.f) + LN_EPS);
    if (tid < 128) ((__attribute__((address_space(3))) float*)(sm + W3_CS))[tid] = cs;
    asm volatile("" : "+v"(mean), "+v"(rstd) :: "memory"); }
  rsrc_t rx = mk_rsrc(ws + WS_X1, 2u << 20), rw = mk_rsrc(w3, D * F * 2);
  const unsigned xv = lane * 16, xo = mt * 32768;
  const int kr = 8 * w + (lane >> 4);
  const unsigned wv = (kr * F + tn * 128) * 2 + (((lane & 15) ^ ((kr & 3) << 2)) << 4);
  const unsigned dx = l0 + w * 2048, dw = l0 + 8192 + w * 2048;
#define W3_STAGE(t) do { const unsigned sl_ = ((t) & 3) * W3_SLOT; dma16(rx, xv, xo + (t) * 2048, dx + sl_); dma16(rx, xv, xo + (t) * 2048 + 1024, dx + sl_ + 1024); \
    dma16(rw, wv, (t) * 32 * F * 2, dw + sl_); dma16(rw, wv, (t) * 32 * F * 2 + 4 * F * 2, dw + sl_ + 1024); } while (0)
  W3_STAGE(0); W3_STAGE(1); W3_STAGE(2); W3_STAGE(3);
  const int q4 = (lane & 15) >> 2, p4 = lane & 3, cb = (lane >> 4) & 1;
  const int wo_ = 8192 + (8 * hh + q4) * 256 + 2 * (16 * (p4 & 1) + 8 * cb + 4 * (p4 >> 1));
  lds_p xa = sm + w * 2048 + lane * 16;
  lds_p wa[4];
#pragma unroll
  for (int nb = 0; nb < 4; ++nb) wa[nb] = sm + wo_ + ((nb ^ q4) << 6);
  f32x16 acc[4] = {};
  for (int s0 = 0; s0 < 16; s0 += 4) {
#pragma unroll
    for (int u = 0; u < 4; ++u) {
      const int s = s0 + u;
      if (s == 0) WAITV_BAR(12); else if (s < 14) WAITV_BAR(8); else if (s == 14) WAITV_BAR(4); else WAITV_BAR(0);
      if (s >= 1 && s + 3 < 16) W3_STAGE(s + 3);
#pragma unroll
      for (int ks = 0; ks < 2; ++ks) {
        u32x4 xf = lds128(xa + u * W3_SLOT + ks * 1024);
#pragma unroll
        for (int nb = 0; nb < 4; ++nb) acc[nb] = mfma(wfrag(wa[nb] + u * W3_SLOT + ks * 4096, 1024), xf, acc[nb]);
      }
    }
  }
#undef W3_STAGE
  rsrc_t ro = mk_rsrc(ws + WS_U, S * F * 2);
  const unsigned o = (mt * 112 + tn * 8 + hh) * 1024 + m * 16;
#pragma unroll
  for (int nb = 0; nb < 4; ++nb) {
    float v[16];
#pragma unroll
    for (int g = 0; g < 4; ++g) {
      f32x4 c = *(__attribute__((address_space(3))) f32x4*)(sm + W3_CS + (nb * 32 + hh * 16 + g * 4) * 4);
#pragma unroll
      for (int i = 0; i < 4; ++i) v[4 * g + i] = gelu(rstd * __builtin_fmaf(-mean, c[i], acc[nb][4 * g + i]));
    }
    st128(ro, o + nb * 2048, pk8(v)); st128(ro, o + nb * 2048 + 512, pk8(v + 8));
  }
}
__global__ void __launch_bounds__(256, 2) w3_kernel(const char* w3, char* ws) {
  __shared__ __attribute__((aligned(16))) char smem[W3_LDS];
  const int b = blockIdx.x, xc = b & 7, i = b >> 3;
  w3_body((lds_p)smem, xc * 2 + (i / 14), i % 14, w3, ws);
}

extern "C" void kernel_launch(void* const* d_in, const int* in_sizes, int n_in, void* d_out, int out_size, void* d_ws, size_t ws_size, hipStream_t stream) {
  const char* x = (const char*)d_in[0]; const char* wqkv = (const char*)d_in[1]; const char* wo = (const char*)d_in[2];
  const char* w1 = (const char*)d_in[4]; const char* w2 = (const char*)d_in[5]; const char* w3 = (const char*)d_in[6]; const char* w4 = (const char*)d_in[7];
  const char* gw = (const char*)d_in[8]; char* ws = (char*)d_ws;
  k1_kernel<<<256, 512, 0, stream>>>(x, wqkv, w1, w3, gw, ws);
  attn_kernel<<<256, 256, 0, stream>>>(ws);
  w2_kernel<<<256, 256, 0, stream>>>(x, w2, ws);
  w3_kernel<<<224, 256, 0, stream>>>(w3, ws);
  fin_kernel<<<256, 256, 0, stream>>>(w4, wo, ws, (char*)d_out);
}
```

```cpp
#include <hip/hip_runtime.h>
#include <hip/hip_bf16.h>

typedef __attribute__((ext_vector_type(8))) __bf16 bf16x8;
typedef __attribute__((ext_vector_type(16))) __bf16 bf16x16;
typedef __attribute__((ext_vector_type(16))) float f32x16;
typedef __attribute__((ext_vector_type(8))) float f32x8;
typedef __attribute__((ext_vector_type(4))) float f32x4;
typedef __attribute__((ext_vector_type(2))) float f32x2;
typedef __attribute__((ext_vector_type(4))) unsigned u32x4;
typedef __attribute__((ext_vector_type(2))) unsigned u32x2;
typedef __attribute__((ext_vector_type(4))) short s16x4;
typedef unsigned long long u64;
#define LDSA __attribute__((address_space(3)))
typedef LDSA char* lds_p;
typedef LDSA f32x4* lds_f4;
typedef LDSA f32x2* lds_f2;
typedef LDSA u32x4* lds_u4;
typedef __attribute__((address_space(1))) u64 gu64;
typedef __amdgpu_buffer_rsrc_t rsrc_t;
#define DI __device__ __forceinline__

constexpr int S = 2048, D = 512, F = 1792;
constexpr unsigned MB2 = 2u << 20;
constexpr size_t WS_Q = 0, WS_K = WS_Q + MB2, WS_V = WS_K + MB2, WS_H = WS_V + MB2, WS_X1 = WS_H + MB2, WS_O = WS_X1 + MB2, WS_U = WS_O + MB2,
                 WS_ST = WS_U + (size_t)S * F * 2,
                 WS_G = WS_ST + (size_t)S * 16 * 8,
                 WS_CS3 = WS_G + S * 4,
                 WS_FL = WS_CS3 + 8 * F * 4;
constexpr int LN = 544, FL_E = 0, FL_Q = 1, FL_H = FL_Q + 24, FL_CS = FL_H + 16, FL_A = FL_CS + 14, FL_B1 = FL_A + 16, FL_B2 = FL_B1 + 16, FL_O = FL_B2 + 16;
constexpr float QSCALE = 0.25f * 1.4426950408889634f;
constexpr float LN_EPS = 1e-5f;
constexpr int SC1 = 16;

DI rsrc_t mk_rsrc(const void* p, unsigned bytes) { return __builtin_amdgcn_make_buffer_rsrc((void*)p, (short)0, (int)bytes, 0x00020000); }
DI unsigned rfl(unsigned x) { return __builtin_amdgcn_readfirstlane(x); }
DI void dma16(rsrc_t r, unsigned voff, unsigned soff, unsigned ldst) {
  asm volatile("s_mov_b32 m0, %0\n\ts_nop 0\n\tbuffer_load_dwordx4 %1, %2, %3 offen lds" ::"s"(ldst), "v"(voff), "s"(r), "s"(soff) : "memory");
}
DI void dma16s(rsrc_t r, unsigned voff, unsigned soff, unsigned ldst) {
  asm volatile("s_mov_b32 m0, %0\n\ts_nop 0\n\tbuffer_load_dwordx4 %1, %2, %3 offen sc1 lds" ::"s"(ldst), "v"(voff), "s"(r), "s"(soff) : "memory");
}
#define WAITV(n) asm volatile("s_waitcnt vmcnt(" #n ")" ::: "memory")
#define WAITV_BAR(n) asm volatile("s_waitcnt vmcnt(" #n ") lgkmcnt(0)\n\ts_barrier" ::: "memory")
#define LBAR() asm volatile("s_waitcnt lgkmcnt(0)\n\ts_barrier" ::: "memory")
#define BAR() asm volatile("s_barrier" ::: "memory")
#define SBAR() __builtin_amdgcn_sched_barrier(0)
#define PRIO(x) __builtin_amdgcn_s_setprio(x)
template <int N> DI void waitv_bar() { asm volatile("s_waitcnt vmcnt(%0)\n\ts_barrier" ::"i"(N) : "memory"); }

DI u32x4 lds128(lds_p p) { return *(lds_u4)p; }
DI u32x2 ldstr(lds_p p) { return __builtin_bit_cast(u32x2, __builtin_amdgcn_ds_read_tr16_b64_v4i16((LDSA s16x4*)p)); }
DI u32x4 wfrag(lds_p p, int rowpitch4) { u32x2 a = ldstr(p), b = ldstr(p + rowpitch4); return (u32x4){a[0], a[1], b[0], b[1]}; }
DI f32x16 mfma(u32x4 a, u32x4 b, f32x16 c) { return __builtin_amdgcn_mfma_f32_32x32x16_bf16(__builtin_bit_cast(bf16x8, a), __builtin_bit_cast(bf16x8, b), c, 0, 0, 0); }
DI f32x4 mfma16(u32x4 a, u32x4 b, f32x4 c) { return __builtin_amdgcn_mfma_f32_16x16x32_bf16(__builtin_bit_cast(bf16x8, a), __builtin_bit_cast(bf16x8, b), c, 0, 0, 0); }
DI f32x16 smf(u32x4 a, f32x8 b, f32x16 c, int idx) { return __builtin_amdgcn_smfmac_f32_32x32x32_bf16(__builtin_bit_cast(bf16x8, a), __builtin_bit_cast(bf16x16, b), c, idx, 0, 0); }
DI f32x8 lo8(f32x16 v) { return __builtin_shufflevector(v, v, 0, 1, 2, 3, 4, 5, 6, 7); }
DI f32x8 hi8(f32x16 v) { return __builtin_shufflevector(v, v, 8, 9, 10, 11, 12, 13, 14, 15); }
DI unsigned pk2(float a, float b) { typedef __attribute__((ext_vector_type(2))) __bf16 b2; b2 r = {(__bf16)a, (__bf16)b}; return __builtin_bit_cast(unsigned, r); }
DI u32x4 pk8(const float* v) { return (u32x4){pk2(v[0], v[1]), pk2(v[2], v[3]), pk2(v[4], v[5]), pk2(v[6], v[7])}; }
DI float bflo(unsigned u) { return __uint_as_float(u << 16); }
DI float bfhi(unsigned u) { return __uint_as_float(u & 0xffff0000u); }
DI float xhalf_sum(float v) { auto r = __builtin_amdgcn_permlane32_swap(__float_as_uint(v), __float_as_uint(v), false, false); return __uint_as_float(r[0]) + __uint_as_float(r[1]); }
DI float xhalf_max(float v) { auto r = __builtin_amdgcn_permlane32_swap(__float_as_uint(v), __float_as_uint(v), false, false); return fmaxf(__uint_as_float(r[0]), __uint_as_float(r[1])); }
DI int swap23(int x) { return (x & ~12) | ((x & 4) << 1) | ((x & 8) >> 1); }
struct LnG { float nmean, sd, A, B; };
DI LnG ln_setup(float sx, float sxx) {
  constexpr float K1 = -2.0f * 0.7978845608028654f * 1.4426950408889634f, K3 = K1 * 0.044715f;
  const float mean = sx * (1.0f / D), var = fmaxf(__builtin_fmaf(-mean, mean, sxx * (1.0f / D)), 0.f) + LN_EPS;
  const float r = __builtin_amdgcn_rsqf(var);
  LnG g; g.nmean = -mean; g.sd = var * r; g.A = K1 * r; g.B = K3 * r * r * r; return g;
}
DI void ln_gelu4(float& a0, float& a1, float& a2, float& a3, f32x4 c, const LnG& g) {
  float p0, p1, p2, p3;
  asm("v_fmac_f32 %0, %8, %12\n\tv_fmac_f32 %1, %9, %12\n\tv_fmac_f32 %2, %10, %12\n\tv_fmac_f32 %3, %11, %12\n\t"
      "v_mul_f32 %4, %0, %0\n\tv_mul_f32 %5, %1, %1\n\tv_mul_f32 %6, %2, %2\n\tv_mul_f32 %7, %3, %3\n\t"
      "v_fma_f32 %4, %4, %15, %14\n\tv_fma_f32 %5, %5, %15, %14\n\tv_fma_f32 %6, %6, %15, %14\n\tv_fma_f32 %7, %7, %15, %14\n\t"
      "v_mul_f32 %4, %4, %0\n\tv_mul_f32 %5, %5, %1\n\tv_mul_f32 %6, %6, %2\n\tv_mul_f32 %7, %7, %3\n\t"
      "v_exp_f32 %4, %4\n\tv_exp_f32 %5, %5\n\tv_exp_f32 %6, %6\n\tv_exp_f32 %7, %7\n\t"
      "v_fma_f32 %4, %4, %13, %13\n\tv_fma_f32 %5, %5, %13, %13\n\tv_fma_f32 %6, %6, %13, %13\n\tv_fma_f32 %7, %7, %13, %13\n\t"
      "v_rcp_f32 %4, %4\n\tv_rcp_f32 %5, %5\n\tv_rcp_f32 %6, %6\n\tv_rcp_f32 %7, %7\n\t"
      "v_mul_f32 %0, %0, %4\n\tv_mul_f32 %1, %1, %5\n\tv_mul_f32 %2, %2, %6\n\tv_mul_f32 %3, %3, %7"
      : "+v"(a0), "+v"(a1), "+v"(a2), "+v"(a3), "=&v"(p0), "=&v"(p1), "=&v"(p2), "=&v"(p3)
      : "v"(c[0]), "v"(c[1]), "v"(c[2]), "v"(c[3]), "v"(g.nmean), "v"(g.sd), "v"(g.A), "v"(g.B));
}
DI void ln_gelu16(float* v, const f32x16& acc, lds_p cs, int gstride, const LnG& g) {
#pragma unroll
  for (int q = 0; q < 4; ++q) {
#pragma unroll
    for (int e = 0; e < 4; ++e) v[4 * q + e] = acc[4 * q + e];
    ln_gelu4(v[4 * q], v[4 * q + 1], v[4 * q + 2], v[4 * q + 3], *(lds_f4)(cs + q * gstride), g); }
}
DI void mfma_fence(f32x16& a) { asm volatile("s_nop 15" : "+v"(a)); }
DI float addf(float a, float b) { float r; asm("v_add_f32 %0, %1, %2" : "=v"(r) : "v"(a), "v"(b)); return r; }
DI void add_bf16(float* v, const f32x16& r, u32x4 pa, u32x4 pb) {
#pragma unroll
  for (int e = 0; e < 4; ++e) { v[2 * e] = addf(r[2 * e], bflo(pa[e])); v[2 * e + 1] = addf(r[2 * e + 1], bfhi(pa[e])); v[8 + 2 * e] = addf(r[8 + 2 * e], bflo(pb[e])); v[9 + 2 * e] = addf(r[9 + 2 * e], bfhi(pb[e])); }
}
DI void acc_stats(unsigned u, float& sa, float& sb, float& qa, float& qb) { float a, b;
  asm volatile("v_lshlrev_b32 %0, 16, %6\n\tv_and_b32 %1, 0xffff0000, %6\n\tv_add_f32 %2, %2, %0\n\tv_add_f32 %3, %3, %1\n\tv_fmac_f32 %4, %0, %0\n\tv_fmac_f32 %5, %1, %1"
               : "=&v"(a), "=&v"(b), "+v"(sa), "+v"(sb), "+v"(qa), "+v"(qb) : "v"(u)); }
template <int AUX> DI void st128(rsrc_t r, unsigned off, u32x4 v) { __builtin_amdgcn_raw_buffer_store_b128(v, r, off, 0, AUX); }
template <int AUX> DI u32x4 ld128(rsrc_t r, unsigned off) { return __builtin_amdgcn_raw_buffer_load_b128(r, off, 0, AUX); }
DI void st_tile(rsrc_t r, unsigned off, const float* v) { st128<SC1>(r, off, pk8(v)); st128<SC1>(r, off + 512, pk8(v + 8)); }

#define STAMP(kid, i) do {} while (0)
#define LT_DECL
#define LT(i)
#define LT_DUMP(kid, n)
#define LD_PRIO 3
#define CO_PRIO 1
#define B_PRIO 2
#define EP_PRIO 2
#define NT0 6
#define POLL_SLEEP 4
#define ONES4 ((u32x4){0x3f803f80u, 0x3f803f80u, 0x3f803f80u, 0x3f803f80u})

constexpr int P0_SLOT = 24576, P0_W = 16384;
constexpr int RING = 65536;
constexpr int EX = 73728, L_GW = EX, L_Z = EX + 1024, L_CS = EX + 2048, L_PF = EX + 3072, L_TAG = EX + 4096, L_EP = L_TAG + 8, L_CNT = L_TAG + 12, LDS_BYTES = EX + 4160;

DI int wperm(int lane) { const int p4 = lane & 3, cb = (lane >> 4) & 1; return 16 * (p4 & 1) + 8 * cb + 4 * (p4 >> 1); }

DI u64 get_tag(lds_p sm) { const u32x2 t = *(volatile LDSA u32x2*)(sm + L_TAG); return ((u64)rfl(t[1]) << 32) | rfl(t[0]); }
DI void publish(char* ws, int f, u64 tag) { if ((threadIdx.x & 63) == 0) __hip_atomic_store((gu64*)(ws + WS_FL) + f, tag, __ATOMIC_RELAXED, __HIP_MEMORY_SCOPE_AGENT); }
DI void wait_tags(char* ws, int f, u64 tag) {
  gu64* p = (gu64*)(ws + WS_FL) + f;
  unsigned spins = 0;
  while (!__all(__hip_atomic_load(p, __ATOMIC_RELAXED, __HIP_MEMORY_SCOPE_AGENT) == tag) && ++spins < (1u << 18)) __builtin_amdgcn_s_sleep(POLL_SLEEP);
  asm volatile("" ::: "memory");
}
DI void poll(char* ws, int f, u64 tag, lds_p sm, int l, unsigned epoch) {
  volatile LDSA unsigned* e = (volatile LDSA unsigned*)(sm + L_EP);
  if (l == 0) { wait_tags(ws, f, tag); *e = epoch; }
  else { while (rfl(*e) < epoch) __builtin_amdgcn_s_sleep(1); }
  asm volatile("" ::: "memory");
}

enum { T_Q = 0, T_QG = 1, T_K = 2, T_V = 3, T_H = 4 };
struct P0F { u32x4 x[2], w0[2], w1[2], g[2]; };

template <int TYPE>
DI void p0_consumer(lds_p sm, int tm, int strip, int wm, int lane, char* ws, int rot) {
  const int rho = lane & 31, hh = lane >> 5, tok = swap23(rho);
  const int row = wm * 32 + tok, fx = (row >> 1) & 7;
  lds_p xa[4];
#pragma unroll
  for (int ks = 0; ks < 4; ++ks) xa[ks] = sm + row * 128 + (((2 * ks + hh) ^ fx) << 4);
  const int q4 = (lane & 15) >> 2, p4 = lane & 3, cb = (lane >> 4) & 1;
  const int n0 = (TYPE == T_V) ? (16 * cb + 8 * (p4 & 1) + 4 * (p4 >> 1)) : wperm(lane);
  const int wo_ = (8 * hh + q4) * 128 + 2 * n0, sw = ((q4 >> 1) & 1) << 6;
  lds_p wa0 = sm + P0_W + wo_ + sw, wa1 = sm + P0_W + wo_ + (sw ^ 64);
  lds_p wh0 = wa0 + 2 * P0_SLOT, wh1 = wa1 + 2 * P0_SLOT;
  asm volatile("" : "+v"(wh0), "+v"(wh1));
  lds_p ga = sm + (((lane & 15) == ((lane >> 4) & 1)) ? L_GW : L_Z) + hh * 16;
  f32x16 acc0 = {}, acc1 = {};
  f32x4 c0 = {}, c1 = {};
  float sa = 0.f, sb = 0.f, qa = 0.f, qb = 0.f;
  const unsigned pat = (((lane >> 4) ^ lane) & 1) ? 0u : 0x3f803f80u;
  const u32x4 sel = {pat, pat, pat, pat};
  auto rd = [&](P0F& f, int s, int u, int i) { const int ks = 2 * u + i, sl = s % 3;
    f.x[i] = lds128(xa[ks] + sl * P0_SLOT);
    f.w0[i] = wfrag((sl == 2 ? wh0 : wa0 + sl * P0_SLOT) + ks * 2048, 512); f.w1[i] = wfrag((sl == 2 ? wh1 : wa1 + sl * P0_SLOT) + ks * 2048, 512);
    if (TYPE == T_QG) f.g[i] = lds128(ga + ((s + rot) & 7) * 128 + ks * 32); };
  auto m0 = [&](const P0F& f, int i) { acc0 = (TYPE == T_V) ? mfma(f.x[i], f.w0[i], acc0) : mfma(f.w0[i], f.x[i], acc0);
    if (TYPE == T_H) { SBAR(); acc_stats(f.x[i][0], sa, sb, qa, qb); SBAR(); c0 = mfma16(f.w0[i], sel, c0); SBAR(); acc_stats(f.x[i][1], sa, sb, qa, qb); } };
  auto m1 = [&](const P0F& f, int i) { acc1 = (TYPE == T_V) ? mfma(f.x[i], f.w1[i], acc1) : mfma(f.w1[i], f.x[i], acc1);
    if (TYPE == T_H) { SBAR(); acc_stats(f.x[i][2], sa, sb, qa, qb); SBAR(); c1 = mfma16(f.w1[i], sel, c1); SBAR(); acc_stats(f.x[i][3], sa, sb, qa, qb); }
    if (TYPE == T_QG) c0 = mfma16(f.g[i], f.x[i], c0); };
  auto step = [&](const P0F& c, P0F& n, int s, int u, bool r) {
    m0(c, 0); SBAR(); if (r) rd(n, s, u, 0); SBAR();
    m1(c, 0); SBAR(); if (r) rd(n, s, u, 1); SBAR();
    m0(c, 1); SBAR(); m1(c, 1); SBAR(); };
  P0F fa, fb;
  LBAR(); rd(fa, 0, 0, 0); rd(fa, 0, 0, 1);
#pragma unroll
  for (int s = 0; s < 8; ++s) {
    step(fa, fb, s, 1, true);
    if (s < 7) LBAR();
    step(fb, fa, s + 1, 0, s < 7);
  }
  STAMP(0 + (TYPE < T_V), 1);
  const int tile = tm * 4 + wm;
  float v[16];
  if (TYPE == T_Q || TYPE == T_QG || TYPE == T_K) {
    rsrc_t ro = mk_rsrc(ws + (TYPE == T_K ? WS_K : WS_Q), MB2);
    const float sc = (TYPE == T_K) ? 1.0f : QSCALE;
    const unsigned o = (strip * 4 + hh) * 65536 + tile * 1024 + tok * 16;
#pragma unroll
    for (int r = 0; r < 16; ++r) v[r] = acc0[r] * sc;
    st_tile(ro, o, v);
#pragma unroll
    for (int r = 0; r < 16; ++r) v[r] = acc1[r] * sc;
    st_tile(ro, o + 131072, v);
    if (TYPE == T_QG && lane < 16) {
      rsrc_t rg = mk_rsrc(ws + WS_G, S * 4);
      __builtin_amdgcn_raw_buffer_store_b32(__float_as_uint(__builtin_amdgcn_rcpf(1.0f + __builtin_amdgcn_exp2f(-1.4426950408889634f * c0[0]))), rg, (tile * 32 + swap23(lane)) * 4, 0, SC1);
      __builtin_amdgcn_raw_buffer_store_b32(__float_as_uint(__builtin_amdgcn_rcpf(1.0f + __builtin_amdgcn_exp2f(-1.4426950408889634f * c0[1]))), rg, (tile * 32 + 16 + swap23(lane)) * 4, 0, SC1); }
  } else if (TYPE == T_V) {
    rsrc_t ro = mk_rsrc(ws + WS_V, MB2);
    const unsigned o = (strip * 4 + (rho >> 4)) * 65536 + tile * 1024 + hh * 256 + (rho & 15) * 16;
#pragma unroll
    for (int r = 0; r < 16; ++r) v[r] = acc0[r];
    st_tile(ro, o, v);
#pragma unroll
    for (int r = 0; r < 16; ++r) v[r] = acc1[r];
    st_tile(ro, o + 131072, v);
  } else {
    rsrc_t ro = mk_rsrc(ws + WS_H, MB2);
    lds_p cs = sm + L_CS + wm * 256;
    if ((lane & 14) == 0) { const int o = (16 * (lane & 1) + 4 * (lane >> 4)) * 4; *(lds_f4)(cs + o) = c0; *(lds_f4)(cs + 128 + o) = c1; }
    asm volatile("s_nop 15" : "+v"(acc0), "+v"(acc1));
    const LnG lg = ln_setup(xhalf_sum(sa + sb), xhalf_sum(qa + qb));
    const int grp = strip >> 1, sh = (grp == 0) ? 1 : (grp == 1) ? -1 : (grp == 2) ? 2 : -2;
    const int dst = tile * 32 + tok + sh, dw = dst & (S - 1);
    const unsigned keep = (dst == dw) ? 0xffffffffu : 0u;
    const unsigned o = (dw >> 5) * 32768 + (strip * 4 + hh) * 1024 + (dw & 31) * 16;
#pragma unroll
    for (int b = 0; b < 2; ++b) { ln_gelu16(v, b ? acc1 : acc0, cs + b * 128 + hh * 16, 32, lg);
      st128<SC1>(ro, o + b * 2048, pk8(v) & keep); st128<SC1>(ro, o + b * 2048 + 512, pk8(v + 8) & keep); }
  }
}

struct Args { const char *x, *wqkv, *wo, *w1, *w2, *w3, *w4, *gw; char *ws, *out; };

DI void phase0(lds_p sm, const Args& a, bool chain, int b, int tm, int type, int strip) {
  const int lane = threadIdx.x & 63, w = rfl(threadIdx.x >> 6);
  char* ws = a.ws;
  const unsigned lds0 = (unsigned)(size_t)sm;
  const int rot = 0;
  if (w >= 4) {
    const int l = w - 4, pc = lane & 7, r8 = lane >> 3;
    rsrc_t rx = mk_rsrc(a.x, S * D * 2);
    const unsigned xve = (tm * 128 + 32 * l + r8) * 1024 + ((pc ^ (r8 >> 1)) << 4), xvo = (tm * 128 + 32 * l + r8) * 1024 + ((pc ^ (4 + (r8 >> 1))) << 4);
    const int ld = (type == 3) ? D : 3 * D, col0 = (type == 3 ? 0 : type * 512) + strip * 64;
    rsrc_t rw = mk_rsrc(type == 3 ? a.w1 : a.wqkv, D * ld * 2);
    const unsigned wv = ((16 * l + r8) * ld + col0) * 2 + ((pc ^ (((r8 >> 1) & 1) << 2)) << 4), w8 = 8 * ld * 2;
    const unsigned dx = lds0 + l * 4096, dw = lds0 + P0_W + l * 2048;
    auto issue = [&](int t) {
      const unsigned sl = (t % 3) * P0_SLOT, k = (t + rot) & 7;
#pragma unroll
      for (int q = 0; q < 4; ++q) { dma16(rx, (q & 1) ? xvo : xve, k * 128 + q * 8192, dx + sl + q * 1024); if (q < 2) dma16(rw, wv, (k * 8 + q) * w8, dw + sl + q * 1024); }
    };
    if (l == 1 && type == 0 && strip == 0) dma16(mk_rsrc(a.gw, D * 2), lane * 16, 0, lds0 + L_GW);
    LT_DECL; LT(0);
    issue(0); issue(1); LT(1);
    waitv_bar<6>(); LT(2);
    issue(2);
    const int cstrip = b % 28, crow = b / 28;
#pragma unroll
    for (int s = 0; s < 7; ++s) {
      if (s < 5) WAITV(6); else if (s == 5) WAITV(9); else { if (chain && l == 0) WAITV(11); else WAITV(3); }
      BAR(); LT(3 + s);
      if (s + 3 < 8) issue(s + 3);
      if (s == 4) {
        rsrc_t r = mk_rsrc(l == 0 ? a.w2 : l == 1 ? a.wo : a.w4, chain ? 0 : (l < 2 ? D * D * 2 : F * D * 2));
        const unsigned o = (l < 2) ? b * 2048 : b * 7168 + (l - 2) * 3072;
        dma16(r, lane * 16, o, lds0 + L_PF); dma16(r, lane * 16, o + 1024, lds0 + L_PF); dma16(r, lane * 16, o + (l < 2 ? 1024 : l == 2 ? 2048 : 3072), lds0 + L_PF);
        if (l == 3) dma16(r, lane * 16, o + 2048, lds0 + L_PF);
      }
      if (s == 5 && chain && l == 0) {
        rsrc_t r3 = mk_rsrc(a.w3, b < 224 ? D * F * 2 : 0);
        const unsigned v = (r8 * F + cstrip * 64) * 2 + ((pc ^ (((r8 >> 1) & 1) << 2)) << 4);
#pragma unroll
        for (int t = 0; t < 8; ++t) dma16(r3, v, (crow * 64 + t * 8) * F * 2, lds0 + 2 * P0_SLOT + t * 1024);
      }
    }
    LT_DUMP(chain ? 5 : 4, 10);
    if (chain && l == 0 && b < 224) {
      WAITV(0);
      lds_p sd = sm + 2 * P0_SLOT;
      const int hh = lane >> 5, q4 = (lane & 15) >> 2, wo_ = (8 * hh + q4) * 128 + 2 * wperm(lane), sw = ((q4 >> 1) & 1) << 6;
      f32x16 c0 = {}, c1 = {};
#pragma unroll
      for (int ks = 0; ks < 4; ++ks) { c0 = mfma(wfrag(sd + wo_ + sw + ks * 2048, 512), ONES4, c0); c1 = mfma(wfrag(sd + wo_ + (sw ^ 64) + ks * 2048, 512), ONES4, c1); }
      if ((lane & 31) == 0) {
        rsrc_t rc = mk_rsrc(ws + WS_CS3, 8 * F * 4);
        const unsigned o = (crow * F + cstrip * 64 + hh * 16) * 4;
#pragma unroll
        for (int g = 0; g < 4; ++g) {
          st128<SC1>(rc, o + g * 16, __builtin_bit_cast(u32x4, (f32x4){c0[4 * g], c0[4 * g + 1], c0[4 * g + 2], c0[4 * g + 3]}));
          st128<SC1>(rc, o + 128 + g * 16, __builtin_bit_cast(u32x4, (f32x4){c1[4 * g], c1[4 * g + 1], c1[4 * g + 2], c1[4 * g + 3]})); }
      }
    }
  } else {
    {
      const int j = (chain ? 32 : 0) + (b >> 3), ty = j >> 4, ld2 = (ty == 3) ? D : 3 * D;
      rsrc_t r = mk_rsrc(ty == 3 ? a.w1 : a.wqkv, D * ld2 * 2);
      const unsigned v = (((j & 7) * 64 + w * 16 + (lane >> 3)) * ld2 + (ty == 3 ? 0 : ty * 512) + (2 * (b & 3) + ((j >> 3) & 1)) * 64) * 2 + (lane & 7) * 16;
      dma16(r, v, 0, lds0 + L_PF); dma16(r, v, 8 * ld2 * 2, lds0 + L_PF);
      dma16(mk_rsrc(a.x, S * D * 2), lane * 16, blockIdx.x * 4096 + w * 1024, lds0 + L_PF);
    }
    if (w == 0) {
      const u64 tag = __hip_atomic_load((gu64*)(ws + WS_FL) + FL_E, __ATOMIC_RELAXED, __HIP_MEMORY_SCOPE_AGENT) + 1;
      *(lds_u4)(sm + L_Z + lane * 16) = (u32x4){0u, 0u, 0u, 0u};
      if (lane == 0) *(lds_u4)(sm + L_TAG) = (u32x4){(unsigned)tag, (unsigned)(tag >> 32), 0u, 0u};
    }
    if (type == 0) { if (strip == 0) p0_consumer<T_QG>(sm, tm, strip, w, lane, ws, rot); else p0_consumer<T_Q>(sm, tm, strip, w, lane, ws, rot); }
    else if (type == 1) p0_consumer<T_K>(sm, tm, strip, w, lane, ws, rot);
    else if (type == 2) p0_consumer<T_V>(sm, tm, strip, w, lane, ws, rot);
    else p0_consumer<T_H>(sm, tm, strip, w, lane, ws, rot);
  }
  WAITV_BAR(0);
}

DI float max16(const f32x16& s) {
  float a = fmaxf(fmaxf(s[0], s[1]), s[2]), b = fmaxf(fmaxf(s[3], s[4]), s[5]);
  a = fmaxf(fmaxf(a, s[6]), s[7]); b = fmaxf(fmaxf(b, s[8]), s[9]); a = fmaxf(fmaxf(a, s[10]), s[11]); b = fmaxf(fmaxf(b, s[12]), s[13]);
  return fmaxf(fmaxf(a, s[14]), fmaxf(b, s[15]));
}
DI f32x16 attn_slow(rsrc_t rk, rsrc_t rv, u32x4 qf, int lane) {
  const int rho = lane & 31, hh = lane >> 5;
  f32x16 o = {}; float m = -INFINITY;
#pragma unroll 1
  for (int t = 0; t < 64; ++t) {
    u32x4 kf = ld128<SC1>(rk, t * 1024 + lane * 16);
    u32x4 v0 = ld128<SC1>(rv, t * 1024 + hh * 256 + (rho & 15) * 16), v1 = ld128<SC1>(rv, t * 1024 + 512 + hh * 256 + (rho & 15) * 16);
    if (rho >= 16) { v0 = ONES4; v1 = ONES4; }
    f32x16 z = {}; f32x16 s = mfma(kf, qf, z);
    const float mn = fmaxf(m, xhalf_max(max16(s))), al = __builtin_amdgcn_exp2f(m - mn);
    m = mn;
#pragma unroll
    for (int r = 0; r < 16; ++r) { o[r] *= al; s[r] = __builtin_amdgcn_exp2f(s[r] - mn); }
    o = smf(v0, lo8(s), o, 0xDDDD); o = smf(v1, hi8(s), o, 0xDDDD);
  }
  return o;
}

DI void attn_body(lds_p sm, int head, int qb, int myflag, char* ws) {
  const int lane = threadIdx.x & 63, w = rfl(threadIdx.x >> 6), qg = w & 3, kh = w >> 2, rho = lane & 31, hh = lane >> 5;
  const unsigned lds0 = (unsigned)(size_t)sm;
  STAMP(2, 0);
  PRIO(3);
  const u64 tag = get_tag(sm);
  if (w == 0) publish(ws, myflag, tag);
  {
    const unsigned cv = ((lane & 11) == 0) ? 0x3f803f80u : 0u;
    if (lane < 32) *(lds_u4)(sm + RING + (w * 2 + (lane >> 4)) * 512 + (lane & 15) * 16) = (u32x4){cv, cv, cv, cv};
  }
  if (w == 7) { const int st = head >> 2;
    wait_tags(ws, lane < 32 ? (FL_Q + 8 + 8 * (lane >> 4) + st) * LN + (lane & 15) : (FL_Q + st) * LN + 2 * qb + (lane & 1), tag); }
  BAR();
  STAMP(2, 1);
  rsrc_t rk = mk_rsrc(ws + WS_K + head * 65536, 65536), rv = mk_rsrc(ws + WS_V + head * 65536, 65536), rq = mk_rsrc(ws + WS_Q + head * 65536, 65536);
  const int qt = qb * 8 + qg * 2;
  u32x4 qA = ld128<SC1>(rq, qt * 1024 + lane * 16), qB = ld128<SC1>(rq, qt * 1024 + 1024 + lane * 16);
  rsrc_t rs = (qg & 2) ? rv : rk;
  const unsigned lv = lane * 16 + (qg & 1) * 4096, ld = lds0 + qg * 4096;
  dma16s(rs, lv, kh * 2048, ld + kh * 2048); dma16s(rs, lv, kh * 2048 + 1024, ld + kh * 2048 + 1024);
#define AT_STAGE(c) do { _Pragma("unroll") for (int q_ = 0; q_ < 4; ++q_) dma16s(rs, lv, (c) * 8192 + q_ * 1024, ld + ((c) & 3) * 16384 + q_ * 1024); } while (0)
  lds_p ka = sm + kh * (NT0 * 1024) + lane * 16;
  lds_p va = (rho < 16) ? sm + 8192 + kh * (NT0 * 1024) + hh * 256 + rho * 16 : sm + RING + (rho - 16) * 16;
  const int vstep = (rho < 16) ? 16384 : 0;
  const int idx = 0xDDDD;
  f32x16 negm, sA, sB = {}, oA = {}, oB = {};
  asm volatile("" : "+v"(qA), "+v"(qB), "+v"(oA), "+v"(oB), "+v"(sB));
  WAITV_BAR(0);
  if (kh) AT_STAGE(1);
  { const u32x4 k0 = lds128(sm + lane * 16); f32x16 z = {}; sA = mfma(k0, qA, z); negm = mfma(k0, qB, z);
    const float m = xhalf_max(fmaxf(max16(sA), max16(negm)));
#pragma unroll
    for (int r = 0; r < 16; ++r) negm[r] = -m; }
  asm volatile("" : "+v"(negm));
  u32x4 kf = lds128(ka), v0 = lds128(va), v1 = lds128(va + 512);
#define E_(r) "v_exp_f32 v" #r ", v" #r "\n\t"
#define SM_(o, a, b) "v_smfmac_f32_32x32x32_bf16 v[" o "], v[" a "], v[" b "], v124\n\t"
#define QK_(s, q) "v_mfma_f32_32x32x16_bf16 v[" s "], v[120:123], v[" q "], v[96:111]\n\t"
#define P1_ "s_setprio 1\n\t"
#define P0_ "s_setprio 0\n\t"
#define T_HEAD(offv1) E_(64) E_(65) P1_ SM_("48:63", "28:31", "88:95") "ds_read_b128 v[28:31], v126 offset:" offv1 "\n\t" E_(66) E_(67) E_(68) QK_("80:95", "116:119") P0_
#define T_TAIL(kaddr, vaddr, w0) "ds_read_b128 v[120:123], " kaddr "\n\t" E_(69) E_(70) E_(71) E_(72) E_(73) w0 P1_ SM_("32:47", "24:27", "64:71") P0_ \
    E_(74) E_(75) E_(76) E_(77) E_(78) E_(79) E_(80) E_(81) "s_waitcnt lgkmcnt(0)\n\t" P1_ SM_("32:47", "28:31", "72:79") E_(82) E_(83) E_(84) QK_("64:79", "112:115") P0_ \
    E_(85) E_(86) E_(87) E_(88) E_(89) P1_ SM_("48:63", "24:27", "80:87") P0_ "ds_read_b128 v[24:27], " vaddr "\n\t" E_(90) E_(91) E_(92) E_(93) E_(94) E_(95)
#define T_FULL(a, b) T_HEAD(a) T_TAIL("v125 offset:" b, "v126 offset:" b, "s_waitcnt lgkmcnt(2)\n\t")
#define AT_IO : "+{v[32:47]}"(oA), "+{v[48:63]}"(oB), "+{v[64:79]}"(sA), "+{v[80:95]}"(sB), "+{v[120:123]}"(kf), "+{v[24:27]}"(v0), "+{v[28:31]}"(v1)
#define AT_IN "{v[96:111]}"(negm), "{v[112:115]}"(qA), "{v[116:119]}"(qB), "{v124}"(idx)
  asm volatile("s_nop 1\n\t" QK_("64:79", "112:115") "s_nop 15" AT_IO : AT_IN);
  PRIO(0);
  STAMP(2, 2);
#define CSTAMP()
#define T_EXTRA T_FULL("512", "1024") T_FULL("1536", "2048") T_FULL("2560", "3072") T_FULL("3584", "4096")
#define T_COMMON T_FULL("4608", "5120") T_HEAD("5632")
  const int bias = kh * ((2 * NT0 - 8) * 1024);
  ka -= bias; if (rho < 16) va -= bias;
#pragma unroll 1
  for (int c = 0; c < 8; ++c) {
    CSTAMP();
    const int dsl = ((c + 1) & 3) - (c & 3);
    lds_p kn = ka + dsl * 16384, vn = va + dsl * vstep;
    asm volatile("s_cmp_lg_u32 %[kh], 0\n\ts_cbranch_scc1 1f\n\t" T_EXTRA "1:\n\t" T_COMMON AT_IO : AT_IN, "{v125}"(ka), "{v126}"(va), [kh] "s"(kh) : "memory", "scc");
    if ((0x2b >> c) & 1) {
      WAITV_BAR(0);
      if (kh && c < 5) { const int n = (c == 0) ? 2 : c + 3; PRIO(LD_PRIO); AT_STAGE(n); AT_STAGE(n + 1); PRIO(0); }
    } else asm volatile("s_waitcnt lgkmcnt(0)" ::: "memory");
    asm volatile(T_TAIL("v22", "v23", "") "s_waitcnt lgkmcnt(0)" AT_IO : AT_IN, "{v22}"(kn + bias), "{v23}"(vn + bias) : "memory");
    ka = kn; va = vn;
  }
  asm volatile("s_nop 1\n\t" SM_("48:63", "28:31", "88:95") "s_nop 15" AT_IO : AT_IN);
  STAMP(2, 3);
  PRIO(3);
  LBAR();
  { lds_f4 p = (lds_f4)(sm + w * 4096 + lane * 16); const f32x16& g = kh ? oA : oB;
    p[0] = (f32x4){g[0], g[1], g[2], g[3]}; p[64] = (f32x4){g[4], g[5], g[6], g[7]}; p[128] = (f32x4){g[8], 0.f, 0.f, 0.f}; }
  LBAR();
  f32x16 o = kh ? oB : oA;
  { lds_f4 p = (lds_f4)(sm + (w ^ 4) * 4096 + lane * 16); const f32x4 a = p[0], b = p[64], c = p[128];
#pragma unroll
    for (int i = 0; i < 4; ++i) { o[i] = addf(o[i], a[i]); o[4 + i] = addf(o[4 + i], b[i]); }
    o[8] += c[0]; }
  float t = 0.f;
#pragma unroll
  for (int r = 0; r < 8; ++r) t = __builtin_fmaf(o[r], 0.f, t);
  const bool ok = (o[8] >= 0x1p-40f) && (o[8] <= 0x1p100f) && (t == 0.f);
  if (__builtin_expect(!__all(ok), 0)) o = attn_slow(rk, rv, kh ? qB : qA, lane);
  float rl;
  asm("v_rcp_f32 %0, %1\n\ts_nop 0" : "=v"(rl) : "v"(o[8]));
  float v[8];
#pragma unroll
  for (int r = 0; r < 8; ++r) asm("v_mul_f32 %0, %1, %2" : "=v"(v[r]) : "v"(o[r]), "v"(rl));
  st128<SC1>(mk_rsrc(ws + WS_O, MB2), (qt + kh) * 32768 + head * 1024 + lane * 16, pk8(v));
  WAITV_BAR(0);
  if (w == 0) publish(ws, (FL_O + qb * 2 + (head >> 4)) * LN + (head & 15), tag);
  STAMP(2, 4);
}

struct Src { rsrc_t rx, rw; unsigned xo, xstep, wv, wo, wstep, w2nd, rot, msk; };
DI void ld_x(const Src& s, int t, unsigned dst, unsigned xv) { const unsigned o = s.xo + ((t + s.rot) & s.msk) * s.xstep; dma16s(s.rx, xv, o, dst + (t & 3) * 16384); dma16s(s.rx, xv, o + 1024, dst + (t & 3) * 16384 + 1024); }
DI void ld_w(const Src& s, int t, unsigned dst) { const unsigned o = s.wo + ((t + s.rot) & s.msk) * s.wstep; dma16(s.rw, s.wv, o, dst + (t & 3) * 16384 + 8192); dma16(s.rw, s.wv, o + s.w2nd, dst + (t & 3) * 16384 + 8192 + 1024); }
DI Src src_small(const char* xp, unsigned xbytes, unsigned xbase, unsigned xmt, const char* wp, unsigned wbytes, unsigned wbase, unsigned wld, int l, int lane, unsigned rot) {
  Src s; s.rx = mk_rsrc(xp, xbytes); s.rw = mk_rsrc(wp, wbytes);
  s.xo = xbase + (l >> 1) * xmt + (l & 1) * 2048; s.xstep = 4096;
  const int r8 = lane >> 3; s.wv = r8 * wld + (((lane & 7) ^ (((r8 >> 1) & 1) << 2)) << 4);
  s.wo = wbase + 16 * l * wld; s.wstep = 64 * wld; s.w2nd = 8 * wld; s.rot = rot; s.msk = 7; return s;
}
DI Src src_large(const char* xp, unsigned xbytes, unsigned xbase, unsigned xmt, const char* wp, unsigned wbytes, unsigned wbase, unsigned wld, int l, int lane, unsigned rot) {
  Src s; s.rx = mk_rsrc(xp, xbytes); s.rw = mk_rsrc(wp, wbytes);
  s.xo = xbase + l * xmt; s.xstep = 2048;
  const int r4 = lane >> 4; s.wv = r4 * wld + (((lane & 15) ^ (r4 << 2)) << 4);
  s.wo = wbase + 8 * l * wld; s.wstep = 32 * wld; s.w2nd = 4 * wld; s.rot = rot; s.msk = 15; return s;
}
template <int NS, bool ST> DI void loader_run(const Src& s, unsigned dst, unsigned xv, char* ws, int kid, unsigned sto, unsigned cso, lds_p stl, lds_p csl) {
  LT_DECL; LT(0);
  ld_x(s, 0, dst, xv); ld_x(s, 1, dst, xv); ld_x(s, 2, dst, xv); ld_x(s, 3, dst, xv); LT(1);
  f32x2 p[8]; float c[4];
  if (ST) {
    rsrc_t rst = mk_rsrc(ws + WS_ST, S * 128), rc = mk_rsrc(ws + WS_CS3, 8 * F * 4);
#pragma unroll
    for (int e = 0; e < 8; ++e) asm volatile("buffer_load_dwordx2 %0, %1, %2, %3 offen sc1" : "=v"(p[e]) : "v"(sto), "s"(rst), "s"(e * S * 8) : "memory");
#pragma unroll
    for (int e = 0; e < 4; ++e) asm volatile("buffer_load_dword %0, %1, %2, %3 offen sc1" : "=v"(c[e]) : "v"(cso), "s"(rc), "s"(e * F * 4) : "memory");
  }
  constexpr int X = ST ? 12 : 0;
  waitv_bar<6 + X>(); LT(2);
  waitv_bar<4 + X>(); LT(3); ld_x(s, 4, dst, xv); ld_w(s, 4, dst);
  waitv_bar<6 + X>(); LT(4); ld_x(s, 5, dst, xv); ld_w(s, 5, dst);
  waitv_bar<8>(); LT(5); ld_x(s, 6, dst, xv); ld_w(s, 6, dst);
  if (ST) {
    asm volatile("" : "+v"(p[0]), "+v"(p[1]), "+v"(p[2]), "+v"(p[3]), "+v"(p[4]), "+v"(p[5]), "+v"(p[6]), "+v"(p[7]), "+v"(c[0]), "+v"(c[1]), "+v"(c[2]), "+v"(c[3]));
    float sx = 0.f, sxx = 0.f;
#pragma unroll
    for (int e = 0; e < 8; ++e) { sx = addf(sx, p[e][0]); sxx = addf(sxx, p[e][1]); }
    *(lds_f2)stl = (f32x2){xhalf_sum(sx), xhalf_sum(sxx)};
    *(LDSA float*)csl = xhalf_sum(addf(addf(c[0], c[1]), addf(c[2], c[3])));
    asm volatile("s_waitcnt lgkmcnt(0)" ::: "memory");
  }
#pragma unroll 1
  for (int t = 4; t < NS - 3; ++t) { waitv_bar<8>(); LT(t + 2); ld_x(s, t + 3, dst, xv); ld_w(s, t + 3, dst); }
  waitv_bar<8>(); LT(NS - 1); waitv_bar<4>(); LT(NS); waitv_bar<0>(); LT(NS + 1);
  LT_DUMP(kid, NS + 2);
}
template <int NS> DI f32x16 cons_small(lds_p sm, int w, int lane, char* ws, int late) {
  const int hh = lane >> 5, q4 = (lane & 15) >> 2;
  lds_p xa = sm + (w & 1) * 4096 + lane * 16;
  lds_p wa = sm + 8192 + (8 * hh + q4) * 128 + 2 * wperm(lane) + (((w >> 1) ^ ((q4 >> 1) & 1)) << 6);
  f32x16 acc = {};
  u32x4 xA[4], wA[4], xB[4], wB[4];
#define RD1(X, W, sl, i) do { X[i] = lds128(xa + (sl) + (i) * 1024); W[i] = wfrag(wa + (sl) + (i) * 2048, 512); } while (0)
#define STEPS(X, W, XN, WN, sl) do { \
    acc = mfma(W[0], X[0], acc); SBAR(); RD1(XN, WN, sl, 0); RD1(XN, WN, sl, 1); SBAR(); \
    acc = mfma(W[1], X[1], acc); SBAR(); RD1(XN, WN, sl, 2); RD1(XN, WN, sl, 3); SBAR(); \
    acc = mfma(W[2], X[2], acc); acc = mfma(W[3], X[3], acc); SBAR(); } while (0)
  LBAR(); RD1(xA, wA, 0, 0); RD1(xA, wA, 0, 1); RD1(xA, wA, 0, 2); RD1(xA, wA, 0, 3);
#pragma unroll 1
  for (int s0 = 0; s0 < NS; s0 += 4) {
    if (s0 == 4 && late >= 0) {
      WAITV(0);
      unsigned n = 0;
      if (lane == 0) n = __hip_atomic_fetch_add((LDSA unsigned*)(sm + L_CNT), 1u, __ATOMIC_RELAXED, __HIP_MEMORY_SCOPE_WORKGROUP);
      if (rfl(n) == 3u) publish(ws, late, get_tag(sm));
    }
    LBAR(); STEPS(xA, wA, xB, wB, 16384);
    LBAR(); STEPS(xB, wB, xA, wA, 32768);
    LBAR(); STEPS(xA, wA, xB, wB, 49152);
    if (s0 + 4 < NS) LBAR();
    STEPS(xB, wB, xA, wA, 0);
  }
#undef RD1
#undef STEPS
  return acc;
}
struct Acc4 { f32x16 a[4]; };
template <int NS> DI void cons_large(Acc4& acc, lds_p sm, int w, int lane) {
  const int hh = lane >> 5, q4 = (lane & 15) >> 2;
  lds_p xa = sm + w * 2048 + lane * 16;
  lds_p wa[4];
#pragma unroll
  for (int nb = 0; nb < 4; ++nb) wa[nb] = sm + 8192 + (8 * hh + q4) * 256 + 2 * wperm(lane) + ((nb ^ q4) << 6);
  u32x4 xA, wA[4], xB, wB[4];
#define STEPL(X, W, XN, WN, sl, u) do { \
    acc.a[0] = mfma(W[0], X, acc.a[0]); SBAR(); XN = lds128(xa + (sl) + (u) * 1024); WN[0] = wfrag(wa[0] + (sl) + (u) * 4096, 1024); SBAR(); \
    acc.a[1] = mfma(W[1], X, acc.a[1]); SBAR(); WN[1] = wfrag(wa[1] + (sl) + (u) * 4096, 1024); WN[2] = wfrag(wa[2] + (sl) + (u) * 4096, 1024); SBAR(); \
    acc.a[2] = mfma(W[2], X, acc.a[2]); SBAR(); WN[3] = wfrag(wa[3] + (sl) + (u) * 4096, 1024); SBAR(); \
    acc.a[3] = mfma(W[3], X, acc.a[3]); SBAR(); } while (0)
  LBAR();
  xA = lds128(xa);
#pragma unroll
  for (int nb = 0; nb < 4; ++nb) wA[nb] = wfrag(wa[nb], 1024);
#pragma unroll 1
  for (int s0 = 0; s0 < NS; s0 += 4) {
    STEPL(xA, wA, xB, wB, 0, 1); LBAR(); STEPL(xB, wB, xA, wA, 16384, 0);
    STEPL(xA, wA, xB, wB, 16384, 1); LBAR(); STEPL(xB, wB, xA, wA, 32768, 0);
    STEPL(xA, wA, xB, wB, 32768, 1); LBAR(); STEPL(xB, wB, xA, wA, 49152, 0);
    STEPL(xA, wA, xB, wB, 49152, 1);
    if (s0 + 4 < NS) LBAR();
    STEPL(xB, wB, xA, wA, 0, 0);
  }
#undef STEPL
}
DI unsigned cblk(int t, int rot) { const int h = t >= 14; int u = t - 14 * h + rot; u -= (u >= 14) ? 14 : 0; return 4 * u + 2 * h; }

DI void chain_body(lds_p sm, const Args& a, int xc, int i, int myflag, int gateflag) {
  const int lane = threadIdx.x & 63, w = rfl(threadIdx.x >> 6), hh = lane >> 5, m = lane & 31;
  char* ws = a.ws;
  const int tmA = xc * 4 + (i >> 3), tnA = i & 7, pA = tmA >> 1;
  const bool hasB = i < 28;
  const int pB = xc * 2 + (i >= 14), tnB = i % 14;
  STAMP(3, 0);
  if (w >= 4) {
    PRIO(LD_PRIO);
    const int l = w - 4;
    const unsigned dst = (unsigned)(size_t)sm + l * 2048, xv = lane * 16;
    const u64 tag = get_tag(sm);
    if (l == 0) { publish(ws, myflag, tag);
      if (gateflag >= 0) publish(ws, gateflag, tag);
      const int b = i * 8 + xc;
      if (b < 224) publish(ws, (FL_CS + ((b % 28) >> 1)) * LN + (b / 28) * 2 + (b & 1), tag); }
    const unsigned r8 = (tnA + 2 * tmA) & 7, rB = (tnB + 8 * pB) & 15;
    { Src s = src_small(ws + WS_H, MB2, tmA * 65536, 32768, a.w2, D * D * 2, tnA * 128, D * 2, l, lane, r8);
      ld_w(s, 0, dst); ld_w(s, 1, dst); ld_w(s, 2, dst); ld_w(s, 3, dst);
      poll(ws, lane >= 16 ? (FL_H + pA) * LN + 8 : (FL_H + ((lane & 8) ? (pA + ((tmA & 1) ? 1 : 15)) & 15 : pA)) * LN + (lane & 7), tag, sm, l, 1);
      loader_run<8, false>(s, dst, xv, ws, 6, 0, 0, sm, sm); }
    if (hasB) {
      Src s = src_large(ws + WS_X1, MB2, pB * 4 * 32768, 32768, a.w3, D * F * 2, tnB * 256, F * 2, l, lane, rB);
      ld_w(s, 0, dst); ld_w(s, 1, dst); ld_w(s, 2, dst);
      BAR();
      if (l == 0) publish(ws, (FL_A + pA) * LN + (tmA & 1) * 8 + tnA, tag);
      ld_w(s, 3, dst);
      poll(ws, ((lane & 16) ? FL_CS + tnB : FL_A + pB) * LN + (lane & 15), tag, sm, l, 2);
      loader_run<16, true>(s, dst, xv, ws, 7, (hh * 8 * S + (pB * 4 + l) * 32 + m) * 8, (hh * 4 * F + tnB * 128 + l * 32 + m) * 4, sm + RING + 512 + l * 512 + lane * 8, sm + RING + (l * 32 + m) * 4);
    }
    { Src s = src_small(ws + WS_U, S * F * 2, tmA * 2 * 112 * 1024, 112 * 1024, a.w4, F * D * 2, tnA * 128, D * 2, l, lane, (r8 * 7) >> 2);
#define CX(t) do { const unsigned o_ = s.xo + cblk(t, s.rot) * 2048, d_ = dst + ((t) & 3) * 16384; dma16s(s.rx, xv, o_, d_); dma16s(s.rx, xv, o_ + 1024, d_ + 1024); } while (0)
#define CW(t) do { const unsigned o_ = s.wo + cblk(t, s.rot) * (32 * D * 2), d_ = dst + ((t) & 3) * 16384 + 8192; dma16(s.rw, s.wv, o_, d_); dma16(s.rw, s.wv, o_ + s.w2nd, d_ + 1024); } while (0)
      CW(0); CW(1); CW(2);
      BAR();
      if (l == 0) publish(ws, hasB ? (FL_B1 + pB) * LN + tnB : (FL_A + pA) * LN + (tmA & 1) * 8 + tnA, tag);
      CW(3);
      if (l == 1 && xc == 0 && i == 31) {
#pragma unroll 1
        for (int g = 0; g < 10; ++g) wait_tags(ws, (FL_Q + g * 4 + (lane >> 4)) * LN + (g < 6 ? lane & 15 : lane & 7), tag);
        publish(ws, FL_E, tag);
      }
      const int fl = pA * LN + min(lane, 13);
      poll(ws, FL_B1 * LN + fl, tag, sm, l, 3);
      CX(0); CX(1); CX(2); CX(3);
      waitv_bar<6>();
      waitv_bar<4>(); CX(4); CW(4);
      waitv_bar<6>(); CX(5); CW(5);
#pragma unroll 1
      for (int t = 3; t < 25; ++t) { waitv_bar<8>(); if (t == 11) poll(ws, FL_B2 * LN + fl, tag, sm, l, 4); CX(t + 3); CW(t + 3); }
      waitv_bar<8>(); waitv_bar<4>(); waitv_bar<0>();
#undef CX
#undef CW
    }
    { Src s = src_small(ws + WS_O, MB2, tmA * 65536, 32768, a.wo, D * D * 2, tnA * 128, D * 2, l, lane, r8);
      ld_w(s, 0, dst); ld_w(s, 1, dst); ld_w(s, 2, dst);
      BAR();
      ld_w(s, 3, dst);
      poll(ws, (FL_O + (tmA >> 2) * 2 + ((lane >> 4) & 1)) * LN + (lane & 15), tag, sm, l, 5);
      loader_run<8, false>(s, dst, xv, ws, 1, 0, 0, sm, sm); }
    return;
  }
  PRIO(CO_PRIO);
  float v[16];
  u32x4 pa, pb;
  const int row = tmA * 64 + (w & 1) * 32 + m, col = tnA * 64 + (w >> 1) * 32 + hh * 16;
  {
    rsrc_t rxin = mk_rsrc(a.x, S * D * 2);
    u32x4 xa = ld128<0>(rxin, row * 1024 + col * 2), xb = ld128<0>(rxin, row * 1024 + col * 2 + 16);
    f32x16 r = cons_small<8>(sm, w, lane, ws, -1);
    PRIO(EP_PRIO);
    mfma_fence(r);
    STAMP(3, 1);
    add_bf16(v, r, xa, xb);
    pa = pk8(v); pb = pk8(v + 8);
    rsrc_t ro = mk_rsrc(ws + WS_X1, MB2);
    const unsigned ofr = (row >> 5) * 32768 + (col >> 4) * 1024 + m * 16;
    st128<SC1>(ro, ofr, pa); st128<SC1>(ro, ofr + 512, pb);
    float sa = 0.f, sb = 0.f, qa = 0.f, qb = 0.f;
#pragma unroll
    for (int e = 0; e < 4; ++e) { acc_stats(pa[e], sa, sb, qa, qb); acc_stats(pb[e], sa, sb, qa, qb); }
    const float sx = xhalf_sum(sa + sb), sxx = xhalf_sum(qa + qb);
    if (hh == 0) __builtin_amdgcn_raw_buffer_store_b64((u32x2){__float_as_uint(sx), __float_as_uint(sxx)}, mk_rsrc(ws + WS_ST, S * 128), ((col >> 5) * S + row) * 8, 0, SC1);
    WAITV_BAR(0);
    STAMP(3, 2);
  }
  if (hasB) {
    const int mt = pB * 4 + w;
    Acc4 acc = {};
    PRIO(B_PRIO);
    cons_large<16>(acc, sm, w, lane);
    PRIO(EP_PRIO);
    asm volatile("s_nop 15" : "+v"(acc.a[0]), "+v"(acc.a[1]), "+v"(acc.a[2]), "+v"(acc.a[3]));
    STAMP(3, 3);
    rsrc_t ro = mk_rsrc(ws + WS_U, S * F * 2);
    const unsigned o = (mt * 112 + tnB * 8 + hh) * 1024 + m * 16;
    const f32x2 st = *(lds_f2)(sm + RING + 512 + w * 512 + lane * 8);
    const LnG lg = ln_setup(st[0], st[1]);
#pragma unroll
    for (int nb = 0; nb < 4; ++nb) {
      ln_gelu16(v, acc.a[nb], sm + RING + (nb * 32 + hh * 16) * 4, 16, lg);
      if (nb == 2) { WAITV_BAR(0); STAMP(3, 4); }
      st_tile(ro, o + nb * 2048, v);
    }
  }
  {
    STAMP(3, 5);
    PRIO(CO_PRIO);
    f32x16 r = cons_small<28>(sm, w, lane, ws, hasB ? (FL_B2 + pB) * LN + tnB : -1);
    PRIO(EP_PRIO);
    mfma_fence(r);
    STAMP(3, 6);
    LBAR();
    add_bf16(v, r, pa, pb);
    const float g = __uint_as_float(__builtin_amdgcn_raw_buffer_load_b32(mk_rsrc(ws + WS_G, S * 4), row * 4, 0, SC1));
    r = cons_small<8>(sm, w, lane, ws, -1);
    STAMP(3, 7);
#pragma unroll
    for (int q = 0; q < 16; ++q) v[q] = __builtin_fmaf(g, r[q] - v[q], v[q]);
    rsrc_t ro = mk_rsrc(a.out, S * D * 2);
    st128<0>(ro, row * 1024 + col * 2, pk8(v)); st128<0>(ro, row * 1024 + col * 2 + 16, pk8(v + 8));
    STAMP(3, 8);
  }
}

__global__ void __launch_bounds__(512, 4) fused_kernel(Args a) {
  __shared__ __attribute__((aligned(16))) char smem[LDS_BYTES];
  lds_p sm = (lds_p)smem;
  const bool chain = blockIdx.x >= 256;
  const int b = blockIdx.x & 255, xc = b & 7, i = b >> 3;
  { char* ws = a.ws; STAMP(chain ? 1 : 0, 0); }
  const int tm = 8 * (xc >> 2) + (i >> 2), type = (chain ? 0 : 2) + ((i >> 1) & 1), strip = 2 * (xc & 3) + (i & 1);
  phase0(sm, a, chain, b, tm, type, strip);
  const int myflag = (type < 3) ? (FL_Q + type * 8 + strip) * LN + tm : (FL_H + tm) * LN + strip;
  if (chain) chain_body(sm, a, xc, i, myflag, (type == 0 && strip == 0) ? (FL_H + tm) * LN + 8 : -1);
  else attn_body(sm, xc * 4 + (i >> 3), i & 7, myflag, a.ws);
}

extern "C" void kernel_launch(void* const* d_in, const int* in_sizes, int n_in, void* d_out, int out_size, void* d_ws, size_t ws_size, hipStream_t stream) {
  Args a;
  a.x = (const char*)d_in[0]; a.wqkv = (const char*)d_in[1]; a.wo = (const char*)d_in[2]; a.w1 = (const char*)d_in[4]; a.w2 = (const char*)d_in[5];
  a.w3 = (const char*)d_in[6]; a.w4 = (const char*)d_in[7]; a.gw = (const char*)d_in[8]; a.ws = (char*)d_ws; a.out = (char*)d_out;
  fused_kernel<<<512, 512, 0, stream>>>(a);
}
```
